```python
import torch

def _softmax(x, axis=-1):
    return torch.softmax(x, dim=axis)
def _gelu(x):
    return torch.nn.functional.gelu(x, approximate="tanh")
def _silu(x):
    return torch.nn.functional.silu(x)
def _sigmoid(x):
    return torch.sigmoid(x)
def _layernorm(x, axis=-1, eps=1e-5):
    m = x.mean(dim=axis, keepdim=True)
    v = x.var(dim=axis, keepdim=True, unbiased=False)
    return (x - m) / torch.sqrt(v + eps)
def _scatter_add(tgt, idx, src, axis=0):
    return tgt.clone().index_add_(axis, idx, src)
def _segment_sum(src, seg, n):
    out = torch.zeros((n,) + src.shape[1:], dtype=src.dtype)
    return out.index_add_(0, seg, src)
def _pad(x, pw):
    flat = tuple(v for pair in reversed(pw) for v in pair)
    return torch.nn.functional.pad(x, flat)

kernel_name = 'gated_mix__mha_block_cached__mixer_channel_mlp__decode'

def setup_inputs():

    _w = (lambda a: (lambda t: t.to(torch.bfloat16) if t.is_floating_point() else t)(torch.as_tensor(a)))
    def _A_setup():
        B, Sq, D = 2, 1, 576
        H, Sk, Dh = 32, 4096, 128
        return {
            "x": _w((lambda *s: torch.randn(*s, dtype=torch.bfloat16) * 0.05)(B, Sq, D)),
            "k_cache": _w((lambda *s: torch.randn(*s, dtype=torch.bfloat16) * 6.4)(B, H, Sk, Dh)),
            "v_cache": _w((lambda *s: torch.randn(*s, dtype=torch.bfloat16) * 1.6)(B, H, Sk, Dh)),
            "wq": _w((lambda *s: torch.randn(*s, dtype=torch.bfloat16) * 6.4)(D, H * Dh) * (D ** -0.5)),
            "wo": _w((lambda *s: torch.randn(*s, dtype=torch.bfloat16) * 1.6)(H * Dh, D) * ((H * Dh) ** -0.5)),
        }
    _A_in = _A_setup()

    def _B_setup():
        B, S, D, F = 2, 1, 576, 4608
        return {
            "x": _w((lambda *s: torch.randn(*s, dtype=torch.bfloat16) * 0.05)(B, S, D)),
            "w1": _w((lambda *s: torch.randn(*s, dtype=torch.bfloat16) * 3.2)(D, F) * (D ** -0.5)),
            "b1": _w((lambda *s: torch.randn(*s, dtype=torch.bfloat16) * 1.6)(F)),
            "w2": _w((lambda *s: torch.randn(*s, dtype=torch.bfloat16) * 0.1)(F, D) * (F ** -0.5)),
            "b2": _w((lambda *s: torch.randn(*s, dtype=torch.bfloat16) * 0.1)(D)),
        }
    _B_in = _B_setup()

    return {
        "A_x": _A_in["x"],
        "A_k_cache": _A_in["k_cache"],
        "A_v_cache": _A_in["v_cache"],
        "A_wq": _A_in["wq"],
        "A_wo": _A_in["wo"],
        "B_x": _B_in["x"],
        "B_w1": _B_in["w1"],
        "B_b1": _B_in["b1"],
        "B_w2": _B_in["w2"],
        "B_b2": _B_in["b2"],
        "gate_w": _w((lambda *s: torch.randn(*s, dtype=torch.bfloat16) * 0.05)(576) * 16.0),
    }

def reference(A_x, A_k_cache, A_v_cache, A_wq, A_wo, B_x, B_w1, B_b1, B_w2, B_b2, gate_w):
    def _A_ref(x, k_cache, v_cache, wq, wo):
        B, Sq, D = x.shape
        H, Dh = 32, 128
        q = (lambda x, *d: x.permute(*d))((x @ wq).reshape(B, Sq, H, Dh), 0, 2, 1, 3)
        scores = torch.einsum("bhqd,bhkd->bhqk", q, k_cache) * (Dh ** -0.5)
        p = _softmax(scores, axis=-1)
        o = torch.einsum("bhqk,bhkd->bhqd", p, v_cache)
        return (lambda x, *d: x.permute(*d))(o, 0, 2, 1, 3).reshape(B, Sq, H * Dh) @ wo

    def _B_ref(x, w1, b1, w2, b2):
        y = _layernorm(x, axis=-1)
        h = _gelu(torch.einsum("bsd,df->bsf", y, w1) + b1)
        return torch.einsum("bsf,fd->bsd", h, w2) + b2

    x = A_x
    g = _sigmoid((x * gate_w).sum(-1, keepdims=True))
    a = _A_ref(x, A_k_cache, A_v_cache, A_wq, A_wo)
    b = _B_ref(x, B_w1, B_b1, B_w2, B_b2)
    return g * a + (1 - g) * b
```

```cpp
#include <hip/hip_runtime.h>
#include <hip/hip_bf16.h>
#include <stdint.h>

#define KV_AUX 2
#define WIN 40
#define KV_INTERLEAVE 1
#define STAMPS 0

namespace {

typedef unsigned u32;
typedef unsigned long long u64;
typedef __attribute__((ext_vector_type(2))) unsigned u32x2;
typedef __attribute__((ext_vector_type(3))) unsigned u32x3;
typedef __attribute__((ext_vector_type(4))) unsigned u32x4;
typedef __attribute__((ext_vector_type(4))) float f32x4;
typedef __attribute__((ext_vector_type(8))) __bf16 bf16x8;
typedef __attribute__((ext_vector_type(2))) __bf16 bf16x2;
typedef __amdgpu_buffer_rsrc_t rsrc_t;

constexpr int D = 576, H = 32, SK = 4096, DH = 128, F = 4608;
constexpr int NWG = 256, NTHR = 512;
constexpr u32 OOB = 0x80000000u;
constexpr int SC1 = 16;

constexpr u32 WS_NONCE = 0;
constexpr u32 WS_REC1 = 4096, REC1_STRIDE = 1536, REC1_ML = 1024, REC1_H = 1152;
constexpr u32 WS_REC2 = WS_REC1 + NWG * REC1_STRIDE;
constexpr u32 WS_END = WS_REC2 + 8 * 18 * 32 * 64;
constexpr u32 WS_STAMP = 1u << 20;
constexpr u32 SPIN_CAP = 1u << 18;

__device__ __forceinline__ rsrc_t mk(const void* p, u32 bytes) {
  return __builtin_amdgcn_make_buffer_rsrc(const_cast<void*>(p), 0, bytes, 0x00020000);
}
template <int AUX> __device__ __forceinline__ u32x4 ld16(rsrc_t r, u32 v, u32 s) { return __builtin_amdgcn_raw_buffer_load_b128(r, v, s, AUX); }
template <int AUX> __device__ __forceinline__ u32x3 ld12(rsrc_t r, u32 v, u32 s) { return __builtin_amdgcn_raw_buffer_load_b96(r, v, s, AUX); }
template <int AUX> __device__ __forceinline__ u32x2 ld8(rsrc_t r, u32 v, u32 s) { return __builtin_amdgcn_raw_buffer_load_b64(r, v, s, AUX); }
template <int AUX> __device__ __forceinline__ u32 ld2(rsrc_t r, u32 v, u32 s) { return (u32)__builtin_amdgcn_raw_buffer_load_b16(r, v, s, AUX); }
__device__ __forceinline__ float lo(u32 x) { return __builtin_bit_cast(float, x << 16); }
__device__ __forceinline__ float hi(u32 x) { return __builtin_bit_cast(float, x & 0xffff0000u); }
__device__ __forceinline__ float dot2(u32 a, u32 b, float c) {
  return __builtin_amdgcn_fdot2_f32_bf16(__builtin_bit_cast(bf16x2, a), __builtin_bit_cast(bf16x2, b), c, false);
}
__device__ __forceinline__ u32 pk(float a, float b) {
  bf16x2 r = {(__bf16)a, (__bf16)b};
  return __builtin_bit_cast(u32, r);
}
template <int CTRL> __device__ __forceinline__ float dpp(float x) {
  return __builtin_bit_cast(float, __builtin_amdgcn_mov_dpp(__builtin_bit_cast(int, x), CTRL, 0xf, 0xf, true));
}
template <int CTRL> __device__ __forceinline__ u32 dppu(u32 x) { return (u32)__builtin_amdgcn_mov_dpp((int)x, CTRL, 0xf, 0xf, true); }
constexpr int XOR1 = 0xB1, XOR2 = 0x4E, XOR7 = 0x141, ROR8 = 0x128, ROR4 = 0x124, BCAST = 0x150;
__device__ __forceinline__ float x16_sum(float x) {
  auto s = __builtin_amdgcn_permlane16_swap(__float_as_uint(x), __float_as_uint(x), false, false);
  return __uint_as_float(s[0]) + __uint_as_float(s[1]);
}
__device__ __forceinline__ float x32_sum(float x) {
  auto s = __builtin_amdgcn_permlane32_swap(__float_as_uint(x), __float_as_uint(x), false, false);
  return __uint_as_float(s[0]) + __uint_as_float(s[1]);
}
__device__ __forceinline__ float x16_max(float x) {
  auto s = __builtin_amdgcn_permlane16_swap(__float_as_uint(x), __float_as_uint(x), false, false);
  return fmaxf(__uint_as_float(s[0]), __uint_as_float(s[1]));
}
__device__ __forceinline__ float x32_max(float x) {
  auto s = __builtin_amdgcn_permlane32_swap(__float_as_uint(x), __float_as_uint(x), false, false);
  return fmaxf(__uint_as_float(s[0]), __uint_as_float(s[1]));
}
__device__ __forceinline__ float row_sum(float x) { x += dpp<XOR1>(x); x += dpp<XOR2>(x); x += dpp<XOR7>(x); x += dpp<ROR8>(x); return x; }
__device__ __forceinline__ float row_max(float x) { x = fmaxf(x, dpp<XOR1>(x)); x = fmaxf(x, dpp<XOR2>(x)); x = fmaxf(x, dpp<XOR7>(x)); x = fmaxf(x, dpp<ROR8>(x)); return x; }
__device__ __forceinline__ f32x4 mfma(u32x4 a, u32x4 b, f32x4 c) {
  return __builtin_amdgcn_mfma_f32_16x16x32_bf16(__builtin_bit_cast(bf16x8, a), __builtin_bit_cast(bf16x8, b), c, 0, 0, 0);
}
__device__ __forceinline__ u32x4 diag(u32 dup, u32x4 m) { return u32x4{dup & m[0], dup & m[1], dup & m[2], dup & m[3]}; }
__device__ __forceinline__ u32 duplo(u32 x) { return __builtin_amdgcn_perm(x, x, 0x01000100u); }
__device__ __forceinline__ u32 duphi(u32 x) { return __builtin_amdgcn_perm(x, x, 0x03020302u); }
__device__ __forceinline__ void wg_barrier() {
  asm volatile("s_waitcnt lgkmcnt(0)\n\ts_barrier" ::: "memory");
}
#define FENCE() __builtin_amdgcn_sched_barrier(0)
template <int N> struct IC { static constexpr int v = N; };
template <int B, int E, class Fn> __device__ __forceinline__ void sfor(Fn&& f) {
  if constexpr (B < E) { f(IC<B>{}); sfor<B + 1, E>(f); }
}

struct Args {
  const uint16_t* x; const uint16_t* k; const uint16_t* v; const uint16_t* wq; const uint16_t* wo;
  const uint16_t* w1; const uint16_t* b1; const uint16_t* w2; const uint16_t* b2; const uint16_t* gw;
  uint16_t* out; unsigned char* ws;
};

#define STAMP(i)

constexpr int NQ = 18, NK = 32, NV = 32, NO = 4, N2 = 5;
constexpr int P_K = NQ, P_V = P_K + NK, P_O = P_V + NV, P_2 = P_O + NO, P_END = P_2 + N2;

__global__ __launch_bounds__(NTHR, 2) void fused_decode(Args a) {
  __shared__ __attribute__((aligned(16))) float s_q[8 * 128];
  __shared__ __attribute__((aligned(16))) float s_o[8 * 128];
  __shared__ __attribute__((aligned(16))) float s_out[8 * 144];
  __shared__ __attribute__((aligned(16))) float s_h[8 * 36];
  __shared__ __attribute__((aligned(16))) float s_m[8];
  __shared__ __attribute__((aligned(16))) float s_l[8];

  const int tid = threadIdx.x, lane = tid & 63, g = lane >> 4, c = lane & 15;
  const int w = __builtin_amdgcn_readfirstlane(tid >> 6);
  const int bid = blockIdx.x, head = (bid & 7) * 4 + (bid >> 6), j = (bid >> 3) & 7, b = j >> 2, sp = j & 3;
  const int me = head * 8 + j;

  const rsrc_t rx = mk(a.x, 2 * D * 2), rg = mk(a.gw, D * 2);
  const rsrc_t rq = mk(a.wq + head * DH, D * H * DH * 2 - head * DH * 2);
  const rsrc_t r1 = mk(a.w1, D * F * 2);
  const size_t kvoff = ((size_t)(b * H + head) * SK + sp * 1024) * DH;
  const rsrc_t rk = mk(a.k + kvoff, 1024 * DH * 2), rv = mk(a.v + kvoff, 1024 * DH * 2);
  const rsrc_t ro = mk(a.wo + (size_t)head * DH * D, DH * D * 2);
  const rsrc_t r2 = mk(a.w2 + (size_t)head * 144 * D, 144 * D * 2);
  const rsrc_t rw = mk(a.ws, WS_END);

  const u32 vq = (u32)(72 * w + 18 * g) * (H * DH * 2) + c * 16;
  const u32 vkv = KV_INTERLEAVE ? (u32)tid * 16 : (u32)w * 32768 + lane * 16;
  constexpr u32 KV_STEP = KV_INTERLEAVE ? 8192 : 1024;
  const u32 vo = c < 9 ? (u32)(16 * w + 4 * g) * (D * 2) + j * 144 + c * 16 : OOB;
  const u32 v2 = c < 9 ? (u32)(18 * w + 5 * g) * (D * 2) + j * 144 + c * 16 : OOB;
  const u32 v2t = g < 3 ? v2 : OOB;

  u32x4 buf[P_END];
  auto issue = [&](auto n_) {
    constexpr int n = decltype(n_)::v;
    if constexpr (n < P_K) buf[n] = ld16<0>(rq, vq, n * (H * DH * 2));
    else if constexpr (n < P_V) buf[n] = ld16<KV_AUX>(rk, vkv, (n - P_K) * KV_STEP);
    else if constexpr (n < P_O) buf[n] = ld16<KV_AUX>(rv, vkv, (n - P_V) * KV_STEP);
    else if constexpr (n < P_2) buf[n] = ld16<0>(ro, vo, (n - P_O) * (D * 2));
    else if constexpr (n < P_END) buf[n] = ld16<0>(r2, (n - P_2) < 3 ? v2 : v2t, (n - P_2) * (D * 2));
  };

  u32x3 xq[3], xs[3], gs[3], w1r[5];
  const int hb = lane >> 5, hk = lane & 31, p4 = lane & 3, r4 = lane >> 2;
#pragma unroll
  for (int i = 0; i < 3; ++i) xq[i] = ld12<0>(rx, (u32)(b * D + 72 * w + 18 * g) * 2, i * 12);
  FENCE();
  sfor<0, NQ>(issue);
  FENCE();
#pragma unroll
  for (int i = 0; i < 3; ++i) { xs[i] = ld12<0>(rx, (u32)(hb * D + 18 * hk) * 2, i * 12); gs[i] = ld12<0>(rg, (u32)(18 * hk) * 2, i * 12); }
  const u32x2 xm0 = ld8<0>(rx, (u32)(72 * w + 4 * r4) * 2, 0), xm1 = ld8<0>(rx, (u32)(D + 72 * w + 4 * r4) * 2, 0);
  const u32 vt = r4 < 8 ? (u32)(72 * w + 64 + r4) * 2 : OOB;
  const u32 xt0 = ld2<0>(rx, vt, 0), xt1 = ld2<0>(rx, vt, D * 2);
  {
    const u32 col = (u32)(me * 18 + 6 * p4) * 2;
    const u32 v1 = p4 < 3 ? (u32)(72 * w + 4 * r4) * (F * 2) + col : OOB;
    const u32 v1t = (p4 < 3 && r4 < 8) ? (u32)(72 * w + 64 + r4) * (F * 2) + col : OOB;
#pragma unroll
    for (int i = 0; i < 4; ++i) w1r[i] = ld12<0>(r1, v1, i * (F * 2));
    w1r[4] = ld12<0>(r1, v1t, 0);
  }
  const u32 b1v = ld2<0>(mk(a.b1, F * 2), lane < 36 ? (u32)(me * 18 + (lane >> 1)) * 2 : OOB, 0);
  const u32x2 b2v = ld8<0>(mk(a.b2, D * 2), (u32)(72 * j + 8 * (head % 9) + 4 * (lane & 1)) * 2, 0);
  FENCE();
  sfor<NQ, (WIN < P_END ? WIN : P_END)>(issue);
  FENCE();

  const u32 nonce = *reinterpret_cast<const u32*>(a.ws + WS_NONCE);
  const u32 tag = ((nonce + 1u) * 0x9E3779B1u) ^ 0x7F4A7C15u;
  asm volatile("" ::"s"(tag));

  u32x4 msk;
#pragma unroll
  for (int r = 0; r < 4; ++r) msk[r] = (c & 7) == 2 * r ? 0x0000ffffu : (c & 7) == 2 * r + 1 ? 0xffff0000u : 0u;

  f32x4 acc0 = {0.f, 0.f, 0.f, 0.f}, acc1 = {0.f, 0.f, 0.f, 0.f};
  sfor<0, NQ>([&](auto n_) {
    constexpr int n = decltype(n_)::v;
    const u32 xp = xq[n / 6][(n % 6) / 2];
    const u32x4 A = diag((n & 1) ? duphi(xp) : duplo(xp), msk);
    if constexpr (n & 1) acc1 = mfma(A, buf[n], acc1); else acc0 = mfma(A, buf[n], acc0);
    FENCE();
    issue(IC<n + WIN>{});
    FENCE();
  });
  if (lane < 32) *reinterpret_cast<f32x4*>(&s_q[w * 128 + 8 * c + 4 * g]) = acc0 + acc1;

  float mean0, mean1, rstd0, rstd1, gate0, gate1;
  {
    float xv[18], s = 0.f, z = 0.f;
#pragma unroll
    for (int i = 0; i < 9; ++i) {
      xv[2 * i] = lo(xs[i / 3][i % 3]); xv[2 * i + 1] = hi(xs[i / 3][i % 3]);
      s += xv[2 * i] + xv[2 * i + 1];
      z = dot2(xs[i / 3][i % 3], gs[i / 3][i % 3], z);
    }
    s = x16_sum(row_sum(s)); z = x16_sum(row_sum(z));
    const float mu = s * (1.f / D);
    float q2 = 0.f;
#pragma unroll
    for (int i = 0; i < 18; ++i) q2 = fmaf(xv[i] - mu, xv[i] - mu, q2);
    q2 = x16_sum(row_sum(q2));
    const float rs = 1.f / sqrtf(q2 * (1.f / D) + 1e-5f);
    const float gt = 1.f / (1.f + __expf(-z));
    auto rl = [](float v, int l) { return __builtin_bit_cast(float, __builtin_amdgcn_readlane(__builtin_bit_cast(int, v), l)); };
    mean0 = rl(mu, 0); mean1 = rl(mu, 32); rstd0 = rl(rs, 0); rstd1 = rl(rs, 32); gate0 = rl(gt, 0); gate1 = rl(gt, 32);
  }
  {
    float h0[6], h1[6];
#pragma unroll
    for (int k = 0; k < 6; ++k) h0[k] = h1[k] = 0.f;
#pragma unroll
    for (int i = 0; i < 5; ++i) {
      const float x0 = i == 4 ? lo(xt0) : (i & 1) ? hi(xm0[(i & 3) >> 1]) : lo(xm0[(i & 3) >> 1]);
      const float x1 = i == 4 ? lo(xt1) : (i & 1) ? hi(xm1[(i & 3) >> 1]) : lo(xm1[(i & 3) >> 1]);
      const float y0 = (x0 - mean0) * rstd0, y1 = (x1 - mean1) * rstd1;
#pragma unroll
      for (int k = 0; k < 6; ++k) {
        const float wv = (k & 1) ? hi(w1r[i][k >> 1]) : lo(w1r[i][k >> 1]);
        h0[k] = fmaf(y0, wv, h0[k]); h1[k] = fmaf(y1, wv, h1[k]);
      }
    }
#pragma unroll
    for (int k = 0; k < 6; ++k) {
      h0[k] += dpp<ROR4>(h0[k]); h0[k] += dpp<ROR8>(h0[k]); h0[k] = x32_sum(x16_sum(h0[k]));
      h1[k] += dpp<ROR4>(h1[k]); h1[k] += dpp<ROR8>(h1[k]); h1[k] = x32_sum(x16_sum(h1[k]));
    }
    if (lane < 3) {
#pragma unroll
      for (int k = 0; k < 6; k += 2)
        *reinterpret_cast<f32x4*>(&s_h[w * 36 + 12 * lane + 2 * k]) = f32x4{h0[k], h1[k], h0[k + 1], h1[k + 1]};
    }
  }
  wg_barrier();
  STAMP(1);

  u32x4 qp;
  {
    const f32x4* sq = reinterpret_cast<const f32x4*>(&s_q[2 * g * 128 + 8 * c]);
    f32x4 qa = sq[0] + sq[32], qb = sq[1] + sq[33];
#pragma unroll
    for (int k = 0; k < 4; ++k) { qa[k] = x32_sum(x16_sum(qa[k])); qb[k] = x32_sum(x16_sum(qb[k])); }
    qp = u32x4{pk(qa[0], qa[1]), pk(qa[2], qa[3]), pk(qb[0], qb[1]), pk(qb[2], qb[3])};
  }
  if (w == 7) {
    float hv = lo(b1v);
    const int l = lane < 36 ? lane : 0;
#pragma unroll
    for (int k = 0; k < 8; ++k) hv += s_h[k * 36 + l];
    const float u = 1.5957691216057308f * (hv + 0.044715f * hv * hv * hv);
    hv = hv / (1.f + __expf(-u));
    const u32 pr = pk(hv, dpp<XOR1>(hv));
    __builtin_amdgcn_raw_buffer_store_b64(u32x2{pr, tag}, rw, (lane < 36 && !(lane & 1)) ? WS_REC1 + me * REC1_STRIDE + REC1_H + (lane >> 1) * 8 : OOB, 0, SC1);
  }
  FENCE();

  float sc[2];
  sfor<0, 2>([&](auto h_) {
    constexpr int hh = decltype(h_)::v;
    float x[16];
    sfor<0, 16>([&](auto i_) {
      constexpr int i = decltype(i_)::v, n = P_K + 16 * hh + i;
      float d = dot2(buf[n][0], qp[0], 0.f);
      d = dot2(buf[n][1], qp[1], d); d = dot2(buf[n][2], qp[2], d); x[i] = dot2(buf[n][3], qp[3], d);
      FENCE();
      issue(IC<n + WIN>{});
      FENCE();
    });
#define TR_STEP(HALF, CTRL) _Pragma("unroll") for (int t = 0; t < (HALF); ++t) { \
      const float t1 = x[t] + dpp<CTRL>(x[t]), t2 = x[t + (HALF)] + dpp<CTRL>(x[t + (HALF)]); x[t] = (c & (HALF)) ? t2 : t1; }
    TR_STEP(8, ROR8) TR_STEP(4, XOR7) TR_STEP(2, XOR2) TR_STEP(1, XOR1)
#undef TR_STEP
    sc[hh] = x[0] * (0.08838834764831845f * 1.4426950408889634f);
    FENCE();
  });
  {
    const float m = x32_max(x16_max(row_max(fmaxf(sc[0], sc[1]))));
    if (lane == 0) s_m[w] = m;
  }
  wg_barrier();
  STAMP(2);
  float M;
  {
    const f32x4 m0 = *reinterpret_cast<const f32x4*>(&s_m[0]), m1 = *reinterpret_cast<const f32x4*>(&s_m[4]);
    M = fmaxf(fmaxf(fmaxf(m0[0], m0[1]), fmaxf(m0[2], m0[3])), fmaxf(fmaxf(m1[0], m1[1]), fmaxf(m1[2], m1[3])));
  }
  u32 pp[2];
  {
    const float p0 = __builtin_amdgcn_exp2f(sc[0] - M), p1 = __builtin_amdgcn_exp2f(sc[1] - M);
    pp[0] = pk(p0, p0); pp[1] = pk(p1, p1);
    const float l = x32_sum(x16_sum(row_sum(p0 + p1)));
    if (lane == 0) s_l[w] = l;
  }
  acc0 = f32x4{0.f, 0.f, 0.f, 0.f}; acc1 = acc0;
  sfor<0, NV>([&](auto i_) {
    constexpr int i = decltype(i_)::v, n = P_V + i;
    const u32x4 A = diag(dppu<BCAST + (i & 15)>(pp[i >> 4]), msk);
    if constexpr (i & 1) acc1 = mfma(A, buf[n], acc1); else acc0 = mfma(A, buf[n], acc0);
    FENCE();
    issue(IC<n + WIN>{});
    FENCE();
  });
  if (lane < 32) *reinterpret_cast<f32x4*>(&s_o[w * 128 + 8 * c + 4 * g]) = acc0 + acc1;
  wg_barrier();
  STAMP(3);
  if (w < 2) {
    float o = 0.f;
#pragma unroll
    for (int k = 0; k < 8; ++k) o += s_o[k * 128 + tid];
    __builtin_amdgcn_raw_buffer_store_b64(u32x2{__float_as_uint(o), tag}, rw, WS_REC1 + me * REC1_STRIDE + tid * 8, 0, SC1);
  } else if (w == 2) {
    const f32x4 l0 = *reinterpret_cast<const f32x4*>(&s_l[0]), l1 = *reinterpret_cast<const f32x4*>(&s_l[4]);
    const float L = ((l0[0] + l0[1]) + (l0[2] + l0[3])) + ((l1[0] + l1[1]) + (l1[2] + l1[3]));
    __builtin_amdgcn_raw_buffer_store_b64(u32x2{__float_as_uint(lane ? L : M), tag}, rw, lane < 2 ? WS_REC1 + me * REC1_STRIDE + REC1_ML + lane * 8 : OOB, 0, SC1);
  }
  FENCE();
  u32x4 og, ml; u32x2 hg;
  {
    const u32 src = WS_REC1 + (u32)(head * 8 + (c >> 3) * 4 + (c & 3)) * REC1_STRIDE;
    const u32 vog = src + (u32)(16 * w + 4 * g + 2 * ((c >> 2) & 1)) * 8, vml = src + REC1_ML;
    const bool hok = c < 5 && 5 * g + c < 18;
    const u32 vhg = hok ? WS_REC1 + (u32)(head * 8 + w) * REC1_STRIDE + REC1_H + (5 * g + c) * 8 : OOB;
    for (u32 spin = 0; spin < SPIN_CAP; ++spin) {
      og = ld16<SC1>(rw, vog, 0); ml = ld16<SC1>(rw, vml, 0); hg = ld8<SC1>(rw, vhg, 0);
      const bool ok = og[1] == tag && og[3] == tag && ml[1] == tag && ml[3] == tag && (hg[1] == tag || !hok);
      if (__all(ok)) break;
      __builtin_amdgcn_s_sleep(1);
      asm volatile("" ::: "memory");
    }
  }
  STAMP(4);
  {
    const float ms = __uint_as_float(ml[0]), ls = __uint_as_float(ml[2]);
    float mx = fmaxf(ms, dpp<XOR1>(ms)); mx = fmaxf(mx, dpp<XOR2>(mx));
    const float f = __builtin_amdgcn_exp2f(ms - mx);
    float lq = f * ls; lq += dpp<XOR1>(lq); lq += dpp<XOR2>(lq);
    const float scl = f / lq;
    float v0 = __uint_as_float(og[0]) * scl, v1 = __uint_as_float(og[2]) * scl;
    v0 += dpp<XOR1>(v0); v0 += dpp<XOR2>(v0); v1 += dpp<XOR1>(v1); v1 += dpp<XOR2>(v1);
    const u32 mine = pk(v0, v1), other = dppu<XOR7>(mine);
    const u32 p01 = (c & 4) ? other : mine, p23 = (c & 4) ? mine : other;
    f32x4 aa = {0.f, 0.f, 0.f, 0.f}, am = aa;
    aa = mfma(diag(duplo(p01), msk), buf[P_O + 0], aa);
    aa = mfma(diag(duphi(p01), msk), buf[P_O + 1], aa);
    aa = mfma(diag(duplo(p23), msk), buf[P_O + 2], aa);
    aa = mfma(diag(duphi(p23), msk), buf[P_O + 3], aa);
    const u32 selb = (c >> 3) ? 0x03020302u : 0x01000100u;
    sfor<0, N2>([&](auto i_) {
      constexpr int i = decltype(i_)::v;
      const u32 hp = dppu<BCAST + i>(hg[0]);
      am = mfma(diag(__builtin_amdgcn_perm(hp, hp, selb), msk), buf[P_2 + i], am);
    });
    const float gt = (g >> 1) ? gate1 : gate0;
    if (c < 9) *reinterpret_cast<f32x4*>(&s_out[w * 144 + (g >> 1) * 72 + 8 * c + 4 * (g & 1)]) = gt * aa + (1.f - gt) * am;
  }
  wg_barrier();
  if (w < 3) {
    const int t = tid < 144 ? tid : 0;
    float o = 0.f;
#pragma unroll
    for (int k = 0; k < 8; ++k) o += s_out[k * 144 + t];
    __builtin_amdgcn_raw_buffer_store_b64(u32x2{__float_as_uint(o), tag}, rw,
        tid < 144 ? WS_REC2 + (u32)((j * 18 + (tid >> 3)) * 32 + head) * 64 + (tid & 7) * 8 : OOB, 0, SC1);
  }
  STAMP(5);
  if (w == 3 && head < 18) {
    const u32 vr = WS_REC2 + (u32)(j * 18 + head) * 2048 + lane * 32;
    u32x4 ra, rb;
    for (u32 spin = 0; spin < SPIN_CAP; ++spin) {
      ra = ld16<SC1>(rw, vr, 0); rb = ld16<SC1>(rw, vr, 16);
      const bool ok = ra[1] == tag && ra[3] == tag && rb[1] == tag && rb[3] == tag;
      if (__all(ok)) break;
      __builtin_amdgcn_s_sleep(1);
      asm volatile("" ::: "memory");
    }
    float v[4] = {__uint_as_float(ra[0]), __uint_as_float(ra[2]), __uint_as_float(rb[0]), __uint_as_float(rb[2])};
    const float og1 = 1.f - (head >= 9 ? gate1 : gate0);
#pragma unroll
    for (int k = 0; k < 4; ++k) {
      v[k] += dpp<XOR2>(v[k]); v[k] += dpp<ROR4>(v[k]); v[k] += dpp<ROR8>(v[k]); v[k] = x32_sum(x16_sum(v[k]));
      v[k] = fmaf(og1, (k & 1) ? hi(b2v[k >> 1]) : lo(b2v[k >> 1]), v[k]);
    }
    if (lane < 2)
      *reinterpret_cast<u32x2*>(a.out + (head / 9) * D + 72 * j + 8 * (head % 9) + 4 * lane) = u32x2{pk(v[0], v[1]), pk(v[2], v[3])};
    if (me == 0 && lane == 0) *reinterpret_cast<u32*>(a.ws + WS_NONCE) = nonce + 1u;
  }
}

}

extern "C" void kernel_launch(void* const* d_in, const int* in_sizes, int n_in, void* d_out, int out_size,
                              void* d_ws, size_t ws_size, hipStream_t stream) {
  Args a;
  a.x = (const uint16_t*)d_in[0]; a.k = (const uint16_t*)d_in[1]; a.v = (const uint16_t*)d_in[2];
  a.wq = (const uint16_t*)d_in[3]; a.wo = (const uint16_t*)d_in[4];
  a.w1 = (const uint16_t*)d_in[6]; a.b1 = (const uint16_t*)d_in[7]; a.w2 = (const uint16_t*)d_in[8];
  a.b2 = (const uint16_t*)d_in[9]; a.gw = (const uint16_t*)d_in[10];
  a.out = (uint16_t*)d_out; a.ws = (unsigned char*)d_ws;
  hipLaunchKernelGGL(fused_decode, dim3(NWG), dim3(NTHR), 0, stream, a);
}
```

```cpp
#include <hip/hip_runtime.h>
#include <hip/hip_bf16.h>
#include <stdint.h>

namespace {

typedef unsigned u32;
typedef unsigned long long u64;
typedef __attribute__((ext_vector_type(2))) unsigned u32x2;
typedef __attribute__((ext_vector_type(4))) unsigned u32x4;
typedef __attribute__((ext_vector_type(4))) float f32x4;
typedef __attribute__((ext_vector_type(8))) __bf16 bf16x8;
typedef __attribute__((ext_vector_type(2))) __bf16 bf16x2;
typedef __amdgpu_buffer_rsrc_t rsrc_t;

constexpr int D = 576, H = 32, SK = 4096, DH = 128, F = 4608;
constexpr int NWG = 256, NTHR = 256;
constexpr int WIN = 12;
constexpr int ILV = 6;
constexpr int N2 = 22;
constexpr int N3 = 34;
constexpr int HPOS = 10;
constexpr int GPOS = 36;
constexpr int W2POS = 37;
constexpr int NW2E = 9;
constexpr int WSP = 3;
constexpr int PACE = 141;
constexpr int W2B = 700;
constexpr int POFF = 600;
constexpr int PN = 2;
constexpr int LA3 = 8;
constexpr int RB = 110;
constexpr int LT = 180;
constexpr int LADD = 40;
constexpr int CPF = 18496;
constexpr int SB = 640;
constexpr int SS = 128;
constexpr u32 OOB = 0x80000000u;
constexpr int SC1 = 16, NT = 2;

constexpr u32 WS_NONCE = 0;
constexpr u32 WS_ML = 4096;
constexpr u32 WS_H = WS_ML + NWG * 64;
constexpr u32 WS_O = WS_H + H * 1152;
constexpr u32 WS_R = WS_O + NWG * 512;
constexpr u32 WS_S = WS_R + 8 * 18 * 32 * 64;
constexpr u32 WS_F = WS_S + 32 * SB;
constexpr u32 WS_A = WS_F + 8 * 128;
constexpr u32 WS_END = WS_A + NWG * 8192;
constexpr u32 SPIN_CAP = 1u << 18;

__device__ __forceinline__ rsrc_t mk(const void* p, u32 bytes) {
  return __builtin_amdgcn_make_buffer_rsrc(const_cast<void*>(p), 0, bytes, 0x00020000);
}
template <int AUX> __device__ __forceinline__ u32x4 ld16(rsrc_t r, u32 v, u32 s) { return __builtin_amdgcn_raw_buffer_load_b128(r, v, s, AUX); }
template <int AUX> __device__ __forceinline__ u32x2 ld8(rsrc_t r, u32 v, u32 s) { return __builtin_amdgcn_raw_buffer_load_b64(r, v, s, AUX); }
template <int AUX> __device__ __forceinline__ u32 ld2(rsrc_t r, u32 v, u32 s) { return (u32)__builtin_amdgcn_raw_buffer_load_b16(r, v, s, AUX); }
__device__ __forceinline__ float lo(u32 x) { return __builtin_bit_cast(float, x << 16); }
__device__ __forceinline__ float hi(u32 x) { return __builtin_bit_cast(float, x & 0xffff0000u); }
__device__ __forceinline__ float dot2(u32 a, u32 b, float c) {
  return __builtin_amdgcn_fdot2_f32_bf16(__builtin_bit_cast(bf16x2, a), __builtin_bit_cast(bf16x2, b), c, false);
}
__device__ __forceinline__ u32 pk(float a, float b) {
  bf16x2 r = {(__bf16)a, (__bf16)b};
  return __builtin_bit_cast(u32, r);
}
template <int CTRL> __device__ __forceinline__ float dpp(float x) {
  return __builtin_bit_cast(float, __builtin_amdgcn_mov_dpp(__builtin_bit_cast(int, x), CTRL, 0xf, 0xf, true));
}
template <int CTRL> __device__ __forceinline__ u32 dppu(u32 x) { return (u32)__builtin_amdgcn_mov_dpp((int)x, CTRL, 0xf, 0xf, true); }
constexpr int XOR1 = 0xB1, XOR2 = 0x4E, XOR7 = 0x141, ROR8 = 0x128, BCAST = 0x150;
__device__ __forceinline__ float x16_sum(float x) {
  auto s = __builtin_amdgcn_permlane16_swap(__float_as_uint(x), __float_as_uint(x), false, false);
  return __uint_as_float(s[0]) + __uint_as_float(s[1]);
}
__device__ __forceinline__ float x32_sum(float x) {
  auto s = __builtin_amdgcn_permlane32_swap(__float_as_uint(x), __float_as_uint(x), false, false);
  return __uint_as_float(s[0]) + __uint_as_float(s[1]);
}
__device__ __forceinline__ float x16_max(float x) {
  auto s = __builtin_amdgcn_permlane16_swap(__float_as_uint(x), __float_as_uint(x), false, false);
  return fmaxf(__uint_as_float(s[0]), __uint_as_float(s[1]));
}
__device__ __forceinline__ float x32_max(float x) {
  auto s = __builtin_amdgcn_permlane32_swap(__float_as_uint(x), __float_as_uint(x), false, false);
  return fmaxf(__uint_as_float(s[0]), __uint_as_float(s[1]));
}
__device__ __forceinline__ float row_sum(float x) { x += dpp<XOR1>(x); x += dpp<XOR2>(x); x += dpp<XOR7>(x); x += dpp<ROR8>(x); return x; }
__device__ __forceinline__ float row_max(float x) { x = fmaxf(x, dpp<XOR1>(x)); x = fmaxf(x, dpp<XOR2>(x)); x = fmaxf(x, dpp<XOR7>(x)); x = fmaxf(x, dpp<ROR8>(x)); return x; }
__device__ __forceinline__ f32x4 mfma(u32x4 a, u32x4 b, f32x4 c) {
  return __builtin_amdgcn_mfma_f32_16x16x32_bf16(__builtin_bit_cast(bf16x8, a), __builtin_bit_cast(bf16x8, b), c, 0, 0, 0);
}
__device__ __forceinline__ u32x4 diag(u32 dup, u32x4 m) { return u32x4{dup & m[0], dup & m[1], dup & m[2], dup & m[3]}; }
__device__ __forceinline__ u32 duplo(u32 x) { return __builtin_amdgcn_perm(x, x, 0x01000100u); }
__device__ __forceinline__ u32 duphi(u32 x) { return __builtin_amdgcn_perm(x, x, 0x03020302u); }
__device__ __forceinline__ void wg_barrier() {
  asm volatile("s_waitcnt lgkmcnt(0)\n\ts_barrier" ::: "memory");
}
template <int T> __device__ __forceinline__ void pace(u32 t0) {
  asm volatile("1:\n\ts_memrealtime vcc\n\ts_waitcnt lgkmcnt(0)\n\ts_sub_u32 vcc_lo, vcc_lo, %0\n\ts_cmpk_lt_u32 vcc_lo, %1\n\ts_cbranch_scc1 1b"
               : : "s"(t0), "n"(T) : "vcc", "scc");
}
#define FENCE() __builtin_amdgcn_sched_barrier(0)
template <int N> struct IC { static constexpr int v = N; };
template <int B, int E, class Fn> __device__ __forceinline__ void sfor(Fn&& f) {
  if constexpr (B < E) { f(IC<B>{}); sfor<B + 1, E>(f); }
}

struct Args {
  const uint16_t* x; const uint16_t* k; const uint16_t* v; const uint16_t* wq; const uint16_t* wo;
  const uint16_t* w1; const uint16_t* b1; const uint16_t* w2; const uint16_t* b2; const uint16_t* gw;
  uint16_t* out; unsigned char* ws;
};

constexpr int NA = 9, NK = 64, NV = 64, NW2 = 9, NWO = 8;
constexpr int P_V = NK, P_2 = P_V + NV + NW2E, P_END = P_V + NV + NW2 + NWO;
constexpr int wpos(int e) { return e < NW2E ? P_V + W2POS + e * (WSP + 1) : P_V + NV + e; }
constexpr int widx(int n) { for (int e = 0; e < NW2 + NWO; ++e) if (wpos(e) == n) return e; return -1; }
constexpr int w2idx(int n) { return widx(n) < NW2 ? widx(n) : -1; }
constexpr int vidx(int n) { int v = n - P_V; for (int e = 0; e < NW2E; ++e) v -= wpos(e) < n; return v; }
static_assert(W2POS > GPOS && W2POS + (NW2E - 1) * WSP < NV, "early weight loads sit inside V");
constexpr u32 chunk(int n) { return ((u32)(n >> 1) << 15) + ((u32)(n & 1) << 12) + ((u32)((n >> 1) & 3) << 13); }
constexpr int due(int n) {
  int bytes = 72 * 272 * 2 / 4;
  for (int i = 0; i < n; ++i) bytes += widx(i) >= 0 ? W2B : 1024;
  return (POFF + bytes * PACE / 1024) / 10;
}
constexpr int imax(int a, int b) { return a > b ? a : b; }
constexpr int imin(int a, int b) { return a < b ? a : b; }
constexpr int issued(int n) { return imin(P_END, imax(N3 + n / 3, n + WIN)); }
constexpr bool paced(int n) { return n % PN == 0 && n >= ILV && n < P_END - LA3; }
__device__ __forceinline__ u32 bad2(u32x4 v, u32 tag) { return (v[1] ^ tag) | (v[3] ^ tag); }
__device__ __forceinline__ bool any(u32 bad) { return __builtin_amdgcn_ballot_w64(bad != 0) != 0; }
#define P_REGS "v224", "v225", "v226", "v227"
#define P_LD "buffer_load_dwordx4 v[224:227], %[v], %[rw], 0 offen sc1\n\t"
__device__ __forceinline__ void poll_start(rsrc_t rw, u32 v, u64 need) {
  asm volatile("s_nop 4\n\ts_mov_b64 exec, %[need]\n\t" P_LD "s_mov_b64 exec, -1"
      : : [v] "v"(v), [rw] "s"(rw), [need] "s"(need) : "memory", P_REGS);
}
__device__ __forceinline__ void poll_wait(rsrc_t rw, u32 v, u64 need, u32 tag, u32& x, u32& y) {
  u64 t; u32 cnt = SPIN_CAP;
  asm volatile("s_nop 4\n\ts_mov_b64 exec, %[need]\n\t"
      "1:\n\ts_waitcnt vmcnt(0)\n\t"
      "v_cmp_eq_u32_e32 vcc, %[tag], v225\n\tv_cmp_eq_u32_e64 %[t], %[tag], v227\n\ts_and_b64 exec, vcc, %[t]\n\t"
      "v_mov_b32_e32 %[x], v224\n\tv_mov_b32_e32 %[y], v226\n\t"
      "s_andn2_b64 %[need], %[need], exec\n\ts_mov_b64 exec, %[need]\n\ts_cbranch_scc0 2f\n\t" P_LD
      "s_sub_u32 %[cnt], %[cnt], 1\n\ts_cbranch_scc0 1b\n\t"
      "s_waitcnt vmcnt(0)\n\t"
      "2:\n\ts_mov_b64 exec, -1\n\ts_nop 1"
      : [x] "+v"(x), [y] "+v"(y), [need] "+s"(need), [cnt] "+s"(cnt), [t] "=&s"(t)
      : [v] "v"(v), [rw] "s"(rw), [tag] "s"(tag) : "memory", "vcc", "scc", P_REGS);
}
__device__ __forceinline__ float gelu(float v) {
  const float u = 1.5957691216057308f * (v + 0.044715f * v * v * v);
  return v / (1.f + __expf(-u));
}

__global__ __launch_bounds__(NTHR, 2) void fused_decode(Args a) {
  __shared__ __attribute__((aligned(16))) u32 s_x[864];
  __shared__ __attribute__((aligned(16))) u32 s_qp[64];
  __shared__ __attribute__((aligned(16))) float s_o[4 * 128];
  __shared__ __attribute__((aligned(16))) float s_out[4 * 144];

  const int tid = threadIdx.x, lane = tid & 63, g = lane >> 4, c = lane & 15, bb = c >> 3;
  const int w = __builtin_amdgcn_readfirstlane(tid >> 6);
  const int bid = blockIdx.x, head = (bid & 7) * 4 + (bid >> 6), j = (bid >> 3) & 7, b = j >> 2, sp = j & 3;
  const int me = head * 8 + j;
  const int ha = bid >> 3, ja = bid & 7;
  const u64 entry = __builtin_amdgcn_s_memrealtime();
  u32 t0;

  if (w == 1) {
    const char* pc = reinterpret_cast<const char*>(__builtin_amdgcn_s_getpc() & ~255ull);
    const u32 off = (u32)((bid >> 3) + 32 * lane) * 64u;
    if (off < CPF) asm volatile("global_load_dword v228, %0, %1" : : "v"(off), "s"(pc) : "v228");
  }
  u32 nonce = *(const __attribute__((address_space(4))) u32*)(a.ws + WS_NONCE);
  const int xid = 54 * w + (lane < 54 ? lane : 53);
  const u32x4 xin = *(xid < 144 ? reinterpret_cast<const u32x4*>(a.x) + xid : reinterpret_cast<const u32x4*>(a.gw) + (xid - 144));
  FENCE();
  u32x4 wa[NA], we[2];
  {
    const bool isq = w < 2;
    const u32 stride = isq ? H * DH * 2 : F * 2;
    const rsrc_t ra = mk(isq ? a.wq : a.w1, D * stride);
    const u32 col = isq ? ha * 256 + w * 128 : ha * 288 + (w - 2) * 144;
    const u32 va = (u32)(72 * ja + 18 * g + bb) * stride + col + (c & 7) * 16;
#pragma unroll
    for (int i = 0; i < NA; ++i) wa[i] = ld16<NT>(ra, va, 2 * i * stride);
    const u32 ve = c < 9 && !isq ? (u32)(72 * ja + 18 * g + 2 * c) * stride + col + 128 : OOB;
    we[0] = ld16<NT>(ra, ve, 0); we[1] = ld16<NT>(ra, ve, stride);
  }
  FENCE();

  const size_t kvoff = (size_t)(b * H + head) * SK * DH;
  const rsrc_t rk = mk(a.k + kvoff, SK * DH * 2), rv = mk(a.v + kvoff, SK * DH * 2);
  const rsrc_t ro = mk(a.wo + (size_t)head * DH * D, DH * D * 2);
  const rsrc_t r2 = mk(a.w2 + (size_t)head * 144 * D, 144 * D * 2);
  const rsrc_t rw = mk(a.ws, WS_END);
  const u32 vkv = (u32)tid * 16, skv = (u32)sp << 13;
  const u32 vcol = c < 9 ? (u32)j * 144 + c * 16 : OOB;
  const u32 vo = vcol + (u32)(32 * w + 8 * g) * (D * 2), v2 = vcol + (u32)(36 * w + 8 * g) * (D * 2), v2t = vcol + (u32)(36 * w + 32 + g) * (D * 2);
  const u32 vml = WS_ML + (u32)(head * 8 + b * 4) * 64 + c * 16;
  const u32 vh = WS_H + (u32)head * 1152 + bb * 576 + (18 * w + 4 * g) * 8, vht = WS_H + (u32)head * 1152 + bb * 576 + (18 * w + 16 + (g >> 1)) * 8;

  const int to = 36 * w + (lane < 36 ? lane : 0);
  const u32 vsum = lane < 36 ? WS_S + (u32)(j * 4 + w) * SB + (lane >> 3) * SS + (lane & 7) * 8 : OOB, vflag = WS_F + (u32)j * 128 + w * 8;
  u32x4 buf[P_END], ml, h01, h23; u32x2 h4, fl; u32 pw0, pw1, pw2;
  auto gather_mlh = [&]() { constexpr int X = SC1; ml = ld16<X>(rw, vml, 0); h01 = ld16<X>(rw, vh, 0); h23 = ld16<X>(rw, vh, 16); h4 = ld8<X>(rw, vht, 0); fl = ld8<X>(rw, vflag, 0); };
  auto issue = [&](auto n_) {
    constexpr int n = decltype(n_)::v;
    if constexpr (paced(n)) pace<due(n)>(t0);
    if constexpr (n == P_V + GPOS) {
      pw0 = __builtin_amdgcn_raw_buffer_load_b32(rw, lane == 0 ? WS_O + (u32)me * 512 + w * 128 : OOB, 0, SC1);
      pw1 = __builtin_amdgcn_raw_buffer_load_b32(mk(a.out, 2 * D * 2), tid == 0 && head < 5 ? (u32)(j * 5 + head) * 64 : OOB, 0, SC1);
      pw2 = __builtin_amdgcn_raw_buffer_load_b32(rw, lane < 3 ? WS_R + (u32)j * (32 * 1152) + head * 1152 + (3 * w + lane) * 96 : OOB, 0, SC1);
      gather_mlh();
    }
    if constexpr (n < P_V) buf[n] = ld16<NT>(rk, vkv, skv ^ chunk(n));
    else if constexpr (w2idx(n) == NW2 - 1) buf[n] = ld16<NT>(r2, v2t, 0);
    else if constexpr (w2idx(n) >= 0) buf[n] = ld16<NT>(r2, v2, w2idx(n) * (D * 2));
    else if constexpr (widx(n) >= 0) buf[n] = ld16<NT>(ro, vo, (widx(n) - NW2) * (D * 2));
    else buf[n] = ld16<NT>(rv, vkv, skv ^ chunk(vidx(n)));
  };

  asm volatile("" : "+s"(nonce));
  asm volatile("" : : "s"(entry));
  const u32 tag = ((nonce + 1u) * 0x9E3779B1u) ^ 0x7F4A7C15u;
  asm volatile("" ::"s"(tag));

  if (lane < 54) *reinterpret_cast<u32x4*>(&s_x[xid * 4]) = xin;
  wg_barrier();
  FENCE();
  {
    const u32 now = (u32)__builtin_amdgcn_s_memrealtime();
    t0 = now - (now - (u32)entry > LT ? RB - LADD : RB);
    asm volatile("" : "+s"(t0));
  }
  FENCE();

  u32x4 msk;
#pragma unroll
  for (int r = 0; r < 4; ++r) msk[r] = (c & 7) == 2 * r ? 0x0000ffffu : (c & 7) == 2 * r + 1 ? 0xffff0000u : 0u;

  auto rl = [](float v, int l) { return __builtin_bit_cast(float, __builtin_amdgcn_readlane(__builtin_bit_cast(int, v), l)); };
  u32 ya[9];
#pragma unroll
  for (int i = 0; i < 9; ++i) ya[i] = s_x[bb * 288 + 36 * ja + 9 * g + i];
  if (w >= 2) {
    const int o = (lane >> 5) * 288 + (lane & 31);
    float xv[18], s = 0.f;
#pragma unroll
    for (int i = 0; i < 9; ++i) {
      const u32 xp = s_x[o + 32 * i];
      xv[2 * i] = lo(xp); xv[2 * i + 1] = hi(xp);
      s += xv[2 * i] + xv[2 * i + 1];
    }
    const float mu = x16_sum(row_sum(s)) * (1.f / D);
    float q2 = 0.f, q3 = 0.f;
#pragma unroll
    for (int i = 0; i < 18; i += 2) { q2 = fmaf(xv[i] - mu, xv[i] - mu, q2); q3 = fmaf(xv[i + 1] - mu, xv[i + 1] - mu, q3); }
    const float rs = __builtin_amdgcn_rsqf(x16_sum(row_sum(q2 + q3)) * (1.f / D) + 1e-5f);
    const float m0 = rl(mu, 0), m1 = rl(mu, 32), r0 = rl(rs, 0), r1 = rl(rs, 32);
    const float m_ = bb ? m1 : m0, r_ = bb ? r1 : r0;
#pragma unroll
    for (int i = 0; i < 9; ++i) ya[i] = pk((lo(ya[i]) - m_) * r_, (hi(ya[i]) - m_) * r_);
  }
  FENCE();
  f32x4 acc0 = {0.f, 0.f, 0.f, 0.f}, acc1 = acc0;
  sfor<0, NA>([&](auto i_) {
    constexpr int i = decltype(i_)::v;
    acc0 = mfma(diag(duplo(ya[i]), msk), wa[i], acc0);
    acc1 = mfma(diag(duphi(ya[i]), msk), wa[i], acc1);
    if constexpr (i >= NA - ILV) { FENCE(); issue(IC<i - (NA - ILV)>{}); FENCE(); }
  });
#pragma unroll
  for (int r = 0; r < 4; ++r) acc0[r] += dpp<ROR8>(acc1[r]);
  auto publish_a = [&](int nc) {
    const u32 vs = c < nc ? WS_A + (u32)(ha * 8 + ja) * 8192 + w * 2048 + lane * 16 : OOB;
    __builtin_amdgcn_raw_buffer_store_b128(u32x4{__float_as_uint(acc0[0]), tag, __float_as_uint(acc0[1]), tag}, rw, vs, 0, SC1);
    __builtin_amdgcn_raw_buffer_store_b128(u32x4{__float_as_uint(acc0[2]), tag, __float_as_uint(acc0[3]), tag}, rw, vs, 1024, SC1);
  };
  if (w < 2) {
    publish_a(8);
  } else {
    f32x4 e0 = {0.f, 0.f, 0.f, 0.f}, e1 = e0;
#pragma unroll
    for (int i = 0; i < 9; ++i) {
      const u32x4 z = {0u, 0u, 0u, 0u};
      e0 = mfma(diag(duplo(ya[i]), msk), c == i ? we[0] : z, e0);
      e1 = mfma(diag(duphi(ya[i]), msk), c == i ? we[1] : z, e1);
    }
#pragma unroll
    for (int r = 0; r < 4; ++r) { const float e = row_sum(e0[r] + e1[r]); acc0[r] = c == 8 ? e : acc0[r]; }
    publish_a(9);
  }
  FENCE();
  sfor<ILV, HPOS>(issue);
  FENCE();
  __builtin_amdgcn_raw_buffer_store_b64(u32x2{0u, 0u}, rw, head == 0 ? vsum : OOB, 0, SC1);
  FENCE();
  sfor<HPOS, N2>(issue);
  FENCE();
  const int slot = 5 * w + (lane & 7), sb = slot >= 9, kk = slot - 9 * sb;
  const bool hok = (lane & 7) < 5 && slot < 18;
  u32x4 qa, qb, hp;
  const u32 htag = hok ? tag : 0u;
  const u32 vqg = WS_A + (u32)(head * 8 + g) * 8192 + bb * 2048 + (w & 1) * 1024 + ((2 * b + (w >> 1)) * 16 + (c & 7)) * 16;
  u32 vhg;
  {
    const int u = 18 * j + 2 * kk, uw = u >= 72, uu = u - 72 * uw;
    vhg = hok ? WS_A + (u32)(head * 8 + (lane >> 3)) * 8192 + (2 + uw) * 2048 + ((uu >> 1) & 1) * 1024 + ((2 * sb + ((uu >> 2) & 1)) * 16 + (uu >> 3)) * 16 : OOB;
  }
  auto gather_a = [&]() { constexpr int X = SC1; qa = ld16<X>(rw, vqg, 0); qb = ld16<X>(rw, vqg, 4 * 8192); hp = ld16<X>(rw, vhg, 0); };
  gather_a();
  const u32 b1v = __builtin_amdgcn_raw_buffer_load_b32(mk(a.b1, F * 2), hok ? (u32)(me * 18 + 2 * kk) * 2 : OOB, 0, 0);
  const u32 b2v = ld2<0>(mk(a.b2, D * 2), (u32)(72 * j + to - 72 * (w >> 1)) * 2, 0);
  FENCE();
  constexpr int NG = (N2 / PN + 1) * PN;
  sfor<N2, NG>(issue);
  FENCE();
  float gate0, gate1;
  {
    float z = 0.f;
#pragma unroll
    for (int i = 0; i < 9; ++i) z = dot2(s_x[(lane >> 5) * 288 + (lane & 31) + 32 * i], s_x[576 + (lane & 31) + 32 * i], z);
    const float gt = 1.f / (1.f + __expf(-x16_sum(row_sum(z))));
    gate0 = rl(gt, 0); gate1 = rl(gt, 32);
    asm volatile("" : "+s"(gate0), "+s"(gate1));
  }
  FENCE();
  sfor<NG, N3>(issue);
  FENCE();
  if (any(bad2(qa, tag) | bad2(qb, tag) | bad2(hp, htag))) {
#pragma unroll 1
    for (u32 spin = 0; spin < SPIN_CAP; ++spin) {
      asm volatile("" ::: "memory");
      gather_a();
      if (!any(bad2(qa, tag) | bad2(qb, tag) | bad2(hp, htag))) break;
      __builtin_amdgcn_s_sleep(1);
    }
  }
  {
    const float q0 = x32_sum(x16_sum(__uint_as_float(qa[0]) + __uint_as_float(qb[0])));
    const float q1 = x32_sum(x16_sum(__uint_as_float(qa[2]) + __uint_as_float(qb[2])));
    if (lane < 16) s_qp[c * 4 + w] = pk(q0, q1);
  }
  wg_barrier();
  const u32x4 qp = *reinterpret_cast<const u32x4*>(&s_qp[c * 4]);
  FENCE();

  float sc[4];
  sfor<0, 4>([&](auto t_) {
    constexpr int t = decltype(t_)::v;
    float x[16];
    sfor<0, 16>([&](auto i_) {
      constexpr int i = decltype(i_)::v, n = 16 * t + i;
      float d = dot2(buf[n][0], qp[0], 0.f);
      d = dot2(buf[n][1], qp[1], d); d = dot2(buf[n][2], qp[2], d); x[i] = dot2(buf[n][3], qp[3], d);
      FENCE();
      sfor<issued(n), issued(n + 1)>(issue);
      FENCE();
      if constexpr (n == HPOS) {
        float h0 = __uint_as_float(hp[0]), h1 = __uint_as_float(hp[2]);
        h0 += dpp<ROR8>(h0); h1 += dpp<ROR8>(h1);
        h0 = gelu(x32_sum(x16_sum(h0)) + lo(b1v)); h1 = gelu(x32_sum(x16_sum(h1)) + hi(b1v));
        __builtin_amdgcn_raw_buffer_store_b64(u32x2{pk(h0, h1), tag}, rw, (hok && lane < 8) ? WS_H + (u32)head * 1152 + sb * 576 + (9 * j + kk) * 8 : OOB, 0, SC1);
        __builtin_amdgcn_raw_buffer_store_b64(u32x2{0u, tag}, rw, head == 0 && lane == 0 ? vflag : OOB, 0, SC1);
        FENCE();
      }
    });
#define TR_STEP(HALF, CTRL) _Pragma("unroll") for (int k = 0; k < (HALF); ++k) { \
      const float t1 = x[k] + dpp<CTRL>(x[k]), t2 = x[k + (HALF)] + dpp<CTRL>(x[k + (HALF)]); x[k] = (c & (HALF)) ? t2 : t1; }
    TR_STEP(8, ROR8) TR_STEP(4, XOR7) TR_STEP(2, XOR2) TR_STEP(1, XOR1)
#undef TR_STEP
    sc[t] = x[0] * (0.08838834764831845f * 1.4426950408889634f);
    FENCE();
  });
  u32 pp[4];
  const float mw = x32_max(x16_max(row_max(fmaxf(fmaxf(sc[0], sc[1]), fmaxf(sc[2], sc[3])))));
#pragma unroll
  for (int t = 0; t < 4; ++t) { sc[t] = __builtin_amdgcn_exp2f(sc[t] - mw); pp[t] = pk(sc[t], sc[t]); }
  FENCE();
  float scl;
  u32 hd[5];
  acc0 = f32x4{0.f, 0.f, 0.f, 0.f}; acc1 = acc0;
  f32x4 am = {0.f, 0.f, 0.f, 0.f};
  auto w2mac = [&](auto n_) {
    constexpr int n = decltype(n_)::v, i = w2idx(n);
    am = mfma(diag(i == 8 ? hd[4] : (i & 1) ? duphi(hd[i >> 1]) : duplo(hd[i >> 1]), msk), buf[n], am);
  };
  sfor<P_V, P_2>([&](auto n_) {
    constexpr int n = decltype(n_)::v, i = vidx(n);
    if constexpr (n == P_V + GPOS) {
      if (any(bad2(ml, tag) | bad2(h01, tag) | bad2(h23, tag) | (h4[1] ^ tag) | (fl[1] ^ tag))) {
#pragma unroll 1
        for (u32 spin = 0; spin < SPIN_CAP; ++spin) {
          asm volatile("" ::: "memory");
          gather_mlh();
          if (!any(bad2(ml, tag) | bad2(h01, tag) | bad2(h23, tag) | (h4[1] ^ tag) | (fl[1] ^ tag))) break;
          __builtin_amdgcn_s_sleep(1);
        }
      }
      const float ms = __uint_as_float(ml[0]), ls = __uint_as_float(ml[2]);
      const float mx = row_max(ms);
      scl = __builtin_amdgcn_exp2f(mw - mx) / row_sum(__builtin_amdgcn_exp2f(ms - mx) * ls);
      hd[0] = h01[0]; hd[1] = h01[2]; hd[2] = h23[0]; hd[3] = h23[2]; hd[4] = (g & 1) ? duphi(h4[0]) : duplo(h4[0]);
      asm volatile("" : "+v"(scl), "+v"(hd[4]));
      asm volatile("" : : "v"(pw0), "v"(pw1), "v"(pw2));
      FENCE();
    }
    if constexpr (w2idx(n) >= 0) {
      w2mac(n_);
    } else if constexpr (widx(n) < 0) {
      const u32x4 A = diag(dppu<BCAST + (i & 15)>(pp[i >> 4]), msk);
      if constexpr (i & 1) acc1 = mfma(A, buf[n], acc1); else acc0 = mfma(A, buf[n], acc0);
    }
    FENCE();
    sfor<issued(n), issued(n + 1)>(issue);
    FENCE();
    if constexpr (n == P_V + 3) {
      const float l = x32_sum(x16_sum(row_sum((sc[0] + sc[1]) + (sc[2] + sc[3]))));
      __builtin_amdgcn_raw_buffer_store_b128(u32x4{__float_as_uint(mw), tag, __float_as_uint(l), tag}, rw, lane == 0 ? WS_ML + (u32)me * 64 + w * 16 : OOB, 0, SC1);
      FENCE();
    }
  });
  if (lane < 32) *reinterpret_cast<f32x4*>(&s_o[w * 128 + 8 * c + 4 * g]) = scl * (acc0 + acc1);
  wg_barrier();
  static_assert(issued(P_2) == P_END, "the stream is out before the hand-off");
  {
    const int d0 = 32 * w + 8 * g + (c & 4);
    const bool own = bb * 4 + (c & 3) == j;
    const u32 vog = WS_O + (u32)(head * 8 + bb * 4 + (c & 3)) * 512 + (u32)d0 * 4;
    const f32x4 ov = ((*reinterpret_cast<const f32x4*>(&s_o[d0]) + *reinterpret_cast<const f32x4*>(&s_o[128 + d0])) +
                            (*reinterpret_cast<const f32x4*>(&s_o[256 + d0]) + *reinterpret_cast<const f32x4*>(&s_o[384 + d0])));
    u32 o01 = pk(ov[0], ov[1]), o23 = pk(ov[2], ov[3]);
    __builtin_amdgcn_raw_buffer_store_b128(u32x4{o01, tag, o23, tag}, rw, own ? vog : OOB, 0, SC1);
    FENCE();
    const u64 need = __builtin_amdgcn_ballot_w64(!own);
    poll_start(rw, vog, need);
    FENCE();
    const float gt = (g >> 1) ? gate1 : gate0;
    sfor<P_2, P_V + NV + NW2>(w2mac);
    am = (1.f - gt) * am;
    asm volatile("" : "+v"(am));
    FENCE();
    poll_wait(rw, vog, need, tag, o01, o23);
    float v[4] = {lo(o01), hi(o01), lo(o23), hi(o23)};
#pragma unroll
    for (int k = 0; k < 4; ++k) { v[k] += dpp<XOR1>(v[k]); v[k] += dpp<XOR2>(v[k]); }
    const u32 m0 = pk(v[0], v[1]), m1 = pk(v[2], v[3]), t0 = dppu<XOR7>(m0), t1 = dppu<XOR7>(m1);
    const u32 od[4] = {(c & 4) ? t0 : m0, (c & 4) ? t1 : m1, (c & 4) ? m0 : t0, (c & 4) ? m1 : t1};
    f32x4 aa = {0.f, 0.f, 0.f, 0.f}, aa1 = aa;
    sfor<0, 4>([&](auto i_) {
      constexpr int i = decltype(i_)::v;
      aa = mfma(diag(duplo(od[i]), msk), buf[wpos(NW2 + 2 * i)], aa);
      aa1 = mfma(diag(duphi(od[i]), msk), buf[wpos(NW2 + 2 * i + 1)], aa1);
    });
    if (c < 9) *reinterpret_cast<f32x4*>(&s_out[w * 144 + (g >> 1) * 72 + 8 * c + 4 * (g & 1)]) = gt * (aa + aa1) + am;
  }
  wg_barrier();
  {
    const float o = (s_out[to] + s_out[144 + to]) + (s_out[288 + to] + s_out[432 + to]);
    const bool fits = __builtin_fabsf(o) < 255.f;
    const float os = fits ? fmaf(o, 0x1p18f, 0x1p-16f) : 0.f, oh = __builtin_floorf(os);
    const u32 alo = ((u32)((os - oh) * 0x1p15f) << 17) + (!fits ? 33u * 64 + 1 : __builtin_fabsf(o) < 0x1p-15f ? 65u : 1u);
    const u32 ahi = (u32)((int)oh + (os - oh >= 1.f));
    u32x2 old = {alo, ahi};
    asm volatile("buffer_atomic_add_x2 %0, %1, %2, 0 offen sc0 sc1" : "+v"(old) : "v"(vsum), "s"(rw) : "memory");
    const u32 vraw = WS_R + (u32)j * (32 * 1152) + to * 8;
    __builtin_amdgcn_raw_buffer_store_b64(u32x2{__float_as_uint(o), tag}, rw, lane < 36 ? vraw + head * 1152 : OOB, 0, SC1);
    float bias = (1.f - ((w >> 1) ? gate1 : gate0)) * lo(b2v);
    uint16_t* po = a.out + (w >> 1) * (D - 72) + 72 * j + to;
    asm volatile("" : "+v"(bias), "+v"(po));
    asm volatile("s_waitcnt vmcnt(1)" : "+v"(old) : : "memory");
    if (lane < 36 && (old[0] & 63u) == 31u) {
      const u64 tot = __builtin_bit_cast(u64, old) + (((u64)ahi << 32) | alo);
      float r = fmaf((float)(int)(tot >> 32), 0x1p-18f, (float)((u32)tot >> 17) * 0x1p-33f) + bias;
      if (((u32)tot & 0x1f800u) != 0u) {
        r = bias;
#pragma unroll 1
        for (u32 h = 0, spin = 0; h < H && spin < SPIN_CAP; ++spin) {
          asm volatile("" ::: "memory");
          const u32x2 p = ld8<SC1>(rw, vraw + h * 1152, 0);
          if (p[1] == tag) { r += __uint_as_float(p[0]); ++h; }
        }
      }
      *(__attribute__((address_space(1))) uint16_t*)po = (uint16_t)pk(r, r);
      if (j == 0 && tid == 0) *reinterpret_cast<u32*>(a.ws + WS_NONCE) = nonce + 1u;
    }
  }
}

}

extern "C" void kernel_launch(void* const* d_in, const int* in_sizes, int n_in, void* d_out, int out_size,
                              void* d_ws, size_t ws_size, hipStream_t stream) {
  Args a;
  a.x = (const uint16_t*)d_in[0]; a.k = (const uint16_t*)d_in[1]; a.v = (const uint16_t*)d_in[2];
  a.wq = (const uint16_t*)d_in[3]; a.wo = (const uint16_t*)d_in[4];
  a.w1 = (const uint16_t*)d_in[6]; a.b1 = (const uint16_t*)d_in[7]; a.w2 = (const uint16_t*)d_in[8];
  a.b2 = (const uint16_t*)d_in[9]; a.gw = (const uint16_t*)d_in[10];
  a.out = (uint16_t*)d_out; a.ws = (unsigned char*)d_ws;
  hipLaunchKernelGGL(fused_decode, dim3(NWG), dim3(NTHR), 0, stream, a);
}
```

```cpp
#include <hip/hip_runtime.h>
#include <hip/hip_bf16.h>
#include <stdint.h>

namespace {

typedef unsigned u32;
typedef unsigned long long u64;
typedef __attribute__((ext_vector_type(2))) unsigned u32x2;
typedef __attribute__((ext_vector_type(4))) unsigned u32x4;
typedef __attribute__((ext_vector_type(4))) float f32x4;
typedef __attribute__((ext_vector_type(8))) __bf16 bf16x8;
typedef __attribute__((ext_vector_type(2))) __bf16 bf16x2;
typedef __amdgpu_buffer_rsrc_t rsrc_t;

constexpr int D = 576, H = 32, SK = 4096, DH = 128, F = 4608;
constexpr int NWG = 256, NTHR = 256;
constexpr int WIN = 12;
constexpr int ILV = 8;
constexpr int N2 = 20;
constexpr int N3 = 32;
constexpr int HPOS = 10;
constexpr int GPOS = 36;
constexpr int W2POS = 44;
constexpr int NW2E = 11;
constexpr int WSP = 1;
constexpr int PACE = 141;
constexpr int W2B = 780;
constexpr int POFF = 600;
constexpr int PN = 2;
constexpr int LA3 = 6;
constexpr int RB = 110;
constexpr int LT = 180;
constexpr int LADD = 40;
constexpr int CPF = 18496;
constexpr int SB = 640;
constexpr int SS = 128;
constexpr int EC = 28;
constexpr int LSTEP = 0;
constexpr u32 OOB = 0x80000000u;
constexpr int SC1 = 16, NT = 2;

constexpr u32 WS_NONCE = 0;
constexpr u32 WS_ML = 4096;
constexpr u32 WS_H = WS_ML + NWG * 64;
constexpr u32 WS_O = WS_H + H * 1152;
constexpr u32 WS_R = WS_O + NWG * 512;
constexpr u32 WS_S = WS_R + 8 * 18 * 32 * 64;
constexpr u32 WS_F = WS_S + 32 * SB;
constexpr u32 WS_A = WS_F + 8 * 128;
constexpr u32 WS_END = WS_A + NWG * 8192;
constexpr u32 SPIN_CAP = 1u << 18;

__device__ __forceinline__ rsrc_t mk(const void* p, u32 bytes) {
  return __builtin_amdgcn_make_buffer_rsrc(const_cast<void*>(p), 0, bytes, 0x00020000);
}
template <int AUX> __device__ __forceinline__ u32x4 ld16(rsrc_t r, u32 v, u32 s) { return __builtin_amdgcn_raw_buffer_load_b128(r, v, s, AUX); }
template <int AUX> __device__ __forceinline__ u32x2 ld8(rsrc_t r, u32 v, u32 s) { return __builtin_amdgcn_raw_buffer_load_b64(r, v, s, AUX); }
template <int AUX> __device__ __forceinline__ u32 ld2(rsrc_t r, u32 v, u32 s) { return (u32)__builtin_amdgcn_raw_buffer_load_b16(r, v, s, AUX); }
__device__ __forceinline__ float lo(u32 x) { return __builtin_bit_cast(float, x << 16); }
__device__ __forceinline__ float hi(u32 x) { return __builtin_bit_cast(float, x & 0xffff0000u); }
__device__ __forceinline__ float dot2(u32 a, u32 b, float c) {
  return __builtin_amdgcn_fdot2_f32_bf16(__builtin_bit_cast(bf16x2, a), __builtin_bit_cast(bf16x2, b), c, false);
}
__device__ __forceinline__ u32 pk(float a, float b) {
  bf16x2 r = {(__bf16)a, (__bf16)b};
  return __builtin_bit_cast(u32, r);
}
template <int CTRL> __device__ __forceinline__ float dpp(float x) {
  return __builtin_bit_cast(float, __builtin_amdgcn_mov_dpp(__builtin_bit_cast(int, x), CTRL, 0xf, 0xf, true));
}
template <int CTRL> __device__ __forceinline__ u32 dppu(u32 x) { return (u32)__builtin_amdgcn_mov_dpp((int)x, CTRL, 0xf, 0xf, true); }
constexpr int XOR1 = 0xB1, XOR2 = 0x4E, XOR7 = 0x141, ROR8 = 0x128, BCAST = 0x150;
__device__ __forceinline__ float x16_sum(float x) {
  auto s = __builtin_amdgcn_permlane16_swap(__float_as_uint(x), __float_as_uint(x), false, false);
  return __uint_as_float(s[0]) + __uint_as_float(s[1]);
}
__device__ __forceinline__ float x32_sum(float x) {
  auto s = __builtin_amdgcn_permlane32_swap(__float_as_uint(x), __float_as_uint(x), false, false);
  return __uint_as_float(s[0]) + __uint_as_float(s[1]);
}
__device__ __forceinline__ float x16_max(float x) {
  auto s = __builtin_amdgcn_permlane16_swap(__float_as_uint(x), __float_as_uint(x), false, false);
  return fmaxf(__uint_as_float(s[0]), __uint_as_float(s[1]));
}
__device__ __forceinline__ float x32_max(float x) {
  auto s = __builtin_amdgcn_permlane32_swap(__float_as_uint(x), __float_as_uint(x), false, false);
  return fmaxf(__uint_as_float(s[0]), __uint_as_float(s[1]));
}
__device__ __forceinline__ float row_sum(float x) { x += dpp<XOR1>(x); x += dpp<XOR2>(x); x += dpp<XOR7>(x); x += dpp<ROR8>(x); return x; }
__device__ __forceinline__ float row_max(float x) { x = fmaxf(x, dpp<XOR1>(x)); x = fmaxf(x, dpp<XOR2>(x)); x = fmaxf(x, dpp<XOR7>(x)); x = fmaxf(x, dpp<ROR8>(x)); return x; }
__device__ __forceinline__ f32x4 mfma(u32x4 a, u32x4 b, f32x4 c) {
  return __builtin_amdgcn_mfma_f32_16x16x32_bf16(__builtin_bit_cast(bf16x8, a), __builtin_bit_cast(bf16x8, b), c, 0, 0, 0);
}
__device__ __forceinline__ u32x4 diag(u32 dup, u32x4 m) { return u32x4{dup & m[0], dup & m[1], dup & m[2], dup & m[3]}; }
__device__ __forceinline__ u32 duplo(u32 x) { return __builtin_amdgcn_perm(x, x, 0x01000100u); }
__device__ __forceinline__ u32 duphi(u32 x) { return __builtin_amdgcn_perm(x, x, 0x03020302u); }
__device__ __forceinline__ void wg_barrier() {
  asm volatile("s_waitcnt lgkmcnt(0)\n\ts_barrier" ::: "memory");
}
template <int T> __device__ __forceinline__ void pace(u32 t0) {
  asm volatile("1:\n\ts_memrealtime vcc\n\ts_waitcnt lgkmcnt(0)\n\ts_sub_u32 vcc_lo, vcc_lo, %0\n\ts_cmpk_lt_u32 vcc_lo, %1\n\ts_cbranch_scc1 1b"
               : : "s"(t0), "n"(T) : "vcc", "scc");
}
#define FENCE() __builtin_amdgcn_sched_barrier(0)
template <int N> struct IC { static constexpr int v = N; };
template <int B, int E, class Fn> __device__ __forceinline__ void sfor(Fn&& f) {
  if constexpr (B < E) { f(IC<B>{}); sfor<B + 1, E>(f); }
}

struct Args {
  const uint16_t* x; const uint16_t* k; const uint16_t* v; const uint16_t* wq; const uint16_t* wo;
  const uint16_t* w1; const uint16_t* b1; const uint16_t* w2; const uint16_t* b2; const uint16_t* gw;
  uint16_t* out; unsigned char* ws;
};

constexpr int NA = 9, NK = 64, NV = 64, NW2 = 9, NWO = 8;
constexpr int P_V = NK, P_2 = P_V + NV + NW2E, P_END = P_V + NV + NW2 + NWO;
constexpr int wpos(int e) { return e < NW2E ? P_V + W2POS + e * (WSP + 1) : P_V + NV + e; }
constexpr int widx(int n) { for (int e = 0; e < NW2 + NWO; ++e) if (wpos(e) == n) return e; return -1; }
constexpr int w2idx(int n) { return widx(n) < NW2 ? widx(n) : -1; }
constexpr int vidx(int n) { int v = n - P_V; for (int e = 0; e < NW2E; ++e) v -= wpos(e) < n; return v; }
static_assert(W2POS > GPOS && W2POS + (NW2E - 1) * WSP < NV, "early weight loads sit inside V");
constexpr u32 chunk(int n) { return ((u32)(n >> 1) << 15) + ((u32)(n & 1) << 12) + ((u32)((n >> 1) & 3) << 13); }
constexpr int due(int n) {
  int bytes = 72 * 272 * 2 / 4;
  for (int i = 0; i < n; ++i) bytes += widx(i) >= 0 ? W2B : 1024;
  return (POFF + bytes * PACE / 1024) / 10;
}
constexpr int imax(int a, int b) { return a > b ? a : b; }
constexpr int imin(int a, int b) { return a < b ? a : b; }
constexpr int issued(int n) { return imin(P_END, imax(N3 + n / 3, n + WIN)); }
constexpr bool paced(int n) { return n % PN == 0 && n >= ILV && n < P_END - LA3; }
__device__ __forceinline__ u32 bad2(u32x4 v, u32 tag) { return (v[1] ^ tag) | (v[3] ^ tag); }
__device__ __forceinline__ bool any(u32 bad) { return __builtin_amdgcn_ballot_w64(bad != 0) != 0; }
#define P_REGS "v224", "v225", "v226", "v227"
#define P_LD "buffer_load_dwordx4 v[224:227], %[v], %[rw], 0 offen sc1\n\t"
__device__ __forceinline__ void poll_start(rsrc_t rw, u32 v, u64 need) {
  asm volatile("s_nop 4\n\ts_mov_b64 exec, %[need]\n\t" P_LD "s_mov_b64 exec, -1"
      : : [v] "v"(v), [rw] "s"(rw), [need] "s"(need) : "memory", P_REGS);
}
__device__ __forceinline__ void poll_wait(rsrc_t rw, u32 v, u64 need, u32 tag, u32& x, u32& y) {
  u64 t; u32 cnt = SPIN_CAP;
  asm volatile("s_nop 4\n\ts_mov_b64 exec, %[need]\n\t"
      "1:\n\ts_waitcnt vmcnt(0)\n\t"
      "v_cmp_eq_u32_e32 vcc, %[tag], v225\n\tv_cmp_eq_u32_e64 %[t], %[tag], v227\n\ts_and_b64 exec, vcc, %[t]\n\t"
      "v_mov_b32_e32 %[x], v224\n\tv_mov_b32_e32 %[y], v226\n\t"
      "s_andn2_b64 %[need], %[need], exec\n\ts_mov_b64 exec, %[need]\n\ts_cbranch_scc0 2f\n\t" P_LD
      "s_sub_u32 %[cnt], %[cnt], 1\n\ts_cbranch_scc0 1b\n\t"
      "s_waitcnt vmcnt(0)\n\t"
      "2:\n\ts_mov_b64 exec, -1\n\ts_nop 1"
      : [x] "+v"(x), [y] "+v"(y), [need] "+s"(need), [cnt] "+s"(cnt), [t] "=&s"(t)
      : [v] "v"(v), [rw] "s"(rw), [tag] "s"(tag) : "memory", "vcc", "scc", P_REGS);
}
__device__ __forceinline__ float gelu(float v) {
  const float u = 1.5957691216057308f * (v + 0.044715f * v * v * v);
  return v / (1.f + __expf(-u));
}

__global__ __launch_bounds__(NTHR, 2) void fused_decode(Args a) {
  __shared__ __attribute__((aligned(16))) u32 s_x[864];
  __shared__ __attribute__((aligned(16))) u32 s_qp[64];
  __shared__ __attribute__((aligned(16))) float s_o[4 * 128];
  __shared__ __attribute__((aligned(16))) float s_out[4 * 144];

  const int tid = threadIdx.x, lane = tid & 63, g = lane >> 4, c = lane & 15, bb = c >> 3;
  const int w = __builtin_amdgcn_readfirstlane(tid >> 6);
  const int bid = blockIdx.x, head = (bid & 7) * 4 + (bid >> 6), j = (bid >> 3) & 7, b = j >> 2, sp = j & 3;
  const int me = head * 8 + j;
  const int ha = bid >> 3, ja = bid & 7;
  const u64 entry = __builtin_amdgcn_s_memrealtime();
  u32 t0, slack;

  if (w == 1) {
    const char* pc = reinterpret_cast<const char*>(__builtin_amdgcn_s_getpc() & ~255ull);
    const u32 off = (u32)((bid >> 3) + 32 * lane) * 64u;
    if (off < CPF) asm volatile("global_load_dword v228, %0, %1" : : "v"(off), "s"(pc) : "v228");
  }
  u32 nonce = *(const __attribute__((address_space(4))) u32*)(a.ws + WS_NONCE);
  const int xid = 54 * w + (lane < 54 ? lane : 53);
  const u32x4 xin = *(xid < 144 ? reinterpret_cast<const u32x4*>(a.x) + xid : reinterpret_cast<const u32x4*>(a.gw) + (xid - 144));
  FENCE();
  u32x4 wa[NA], we[2];
  {
    const bool isq = w < 2;
    const u32 stride = isq ? H * DH * 2 : F * 2;
    const rsrc_t ra = mk(isq ? a.wq : a.w1, D * stride);
    const u32 col = isq ? ha * 256 + w * 128 : ha * 288 + (w - 2) * 144;
    const u32 va = (u32)(72 * ja + 18 * g + bb) * stride + col + (c & 7) * 16;
#pragma unroll
    for (int i = 0; i < NA; ++i) wa[i] = ld16<NT>(ra, va, 2 * i * stride);
    const u32 ve = c < 9 && !isq ? (u32)(72 * ja + 18 * g + 2 * c) * stride + col + 128 : OOB;
    we[0] = ld16<NT>(ra, ve, 0); we[1] = ld16<NT>(ra, ve, stride);
  }
  FENCE();

  const size_t kvoff = (size_t)(b * H + head) * SK * DH;
  const rsrc_t rk = mk(a.k + kvoff, SK * DH * 2), rv = mk(a.v + kvoff, SK * DH * 2);
  const rsrc_t ro = mk(a.wo + (size_t)head * DH * D, DH * D * 2);
  const rsrc_t r2 = mk(a.w2 + (size_t)head * 144 * D, 144 * D * 2);
  const rsrc_t rw = mk(a.ws, WS_END);
  const u32 vkv = (u32)tid * 16, skv = (u32)sp << 13;
  const u32 vcol = c < 9 ? (u32)j * 144 + c * 16 : OOB;
  const u32 vo = vcol + (u32)(32 * w + 8 * g) * (D * 2), v2 = vcol + (u32)(36 * w + 8 * g) * (D * 2), v2t = vcol + (u32)(36 * w + 32 + g) * (D * 2);
  const u32 vml = WS_ML + (u32)(head * 8 + b * 4) * 64 + c * 16;
  const u32 vh = WS_H + (u32)head * 1152 + bb * 576 + (18 * w + 4 * g) * 8, vht = WS_H + (u32)head * 1152 + bb * 576 + (18 * w + 16 + (g >> 1)) * 8;

  const int to = 36 * w + (lane < 36 ? lane : 0);
  const u32 vsum = lane < 36 ? WS_S + (u32)(j * 4 + w) * SB + (lane >> 3) * SS + (lane & 7) * 8 : OOB, vflag = WS_F + (u32)j * 128 + w * 8;
  u32x4 buf[P_END], ml, h01, h23; u32x2 h4, fl; u32 pw0, pw1, pw2;
  auto gather_mlh = [&]() { constexpr int X = SC1; ml = ld16<X>(rw, vml, 0); h01 = ld16<X>(rw, vh, 0); h23 = ld16<X>(rw, vh, 16); h4 = ld8<X>(rw, vht, 0); fl = ld8<X>(rw, vflag, 0); };
  auto issue = [&](auto n_) {
    constexpr int n = decltype(n_)::v;
    if constexpr (LSTEP && n % 16 == 0 && n) { t0 += slack; asm volatile("" : "+s"(t0)); }
    if constexpr (paced(n)) pace<due(n)>(t0);
    if constexpr (n == P_V + GPOS) {
      pw0 = __builtin_amdgcn_raw_buffer_load_b32(rw, lane == 0 ? WS_O + (u32)me * 512 + w * 128 : OOB, 0, SC1);
      pw1 = __builtin_amdgcn_raw_buffer_load_b32(mk(a.out, 2 * D * 2), tid == 0 && head < 5 ? (u32)(j * 5 + head) * 64 : OOB, 0, SC1);
      pw2 = __builtin_amdgcn_raw_buffer_load_b32(rw, lane < 3 ? WS_R + (u32)j * (32 * 1152) + head * 1152 + (3 * w + lane) * 96 : OOB, 0, SC1);
      gather_mlh();
    }
    if constexpr (n < P_V) buf[n] = ld16<NT>(rk, vkv, skv ^ chunk(n));
    else if constexpr (w2idx(n) == NW2 - 1) buf[n] = ld16<NT>(r2, v2t, 0);
    else if constexpr (w2idx(n) >= 0) buf[n] = ld16<NT>(r2, v2, w2idx(n) * (D * 2));
    else if constexpr (widx(n) >= 0) buf[n] = ld16<NT>(ro, vo, (widx(n) - NW2) * (D * 2));
    else buf[n] = ld16<NT>(rv, vkv, skv ^ chunk(vidx(n)));
  };

  asm volatile("" : "+s"(nonce));
  asm volatile("" : : "s"(entry));
  const u32 tag = ((nonce + 1u) * 0x9E3779B1u) ^ 0x7F4A7C15u;
  asm volatile("" ::"s"(tag));

  if (lane < 54) *reinterpret_cast<u32x4*>(&s_x[xid * 4]) = xin;
  wg_barrier();
  FENCE();
  {
    const u32 now = (u32)__builtin_amdgcn_s_memrealtime();
    const u32 late = now - (u32)entry;
    t0 = late > LT ? now - (RB - LADD) : (u32)entry + (late >> 1) - EC;
    slack = late > LT ? LSTEP : 0;
    asm volatile("" : "+s"(t0), "+s"(slack));
  }
  FENCE();

  u32x4 msk;
#pragma unroll
  for (int r = 0; r < 4; ++r) msk[r] = (c & 7) == 2 * r ? 0x0000ffffu : (c & 7) == 2 * r + 1 ? 0xffff0000u : 0u;

  auto rl = [](float v, int l) { return __builtin_bit_cast(float, __builtin_amdgcn_readlane(__builtin_bit_cast(int, v), l)); };
  u32 ya[9];
#pragma unroll
  for (int i = 0; i < 9; ++i) ya[i] = s_x[bb * 288 + 36 * ja + 9 * g + i];
  if (w >= 2) {
    const int o = (lane >> 5) * 288 + (lane & 31);
    float xv[18], s = 0.f;
#pragma unroll
    for (int i = 0; i < 9; ++i) {
      const u32 xp = s_x[o + 32 * i];
      xv[2 * i] = lo(xp); xv[2 * i + 1] = hi(xp);
      s += xv[2 * i] + xv[2 * i + 1];
    }
    const float mu = x16_sum(row_sum(s)) * (1.f / D);
    float q2 = 0.f, q3 = 0.f;
#pragma unroll
    for (int i = 0; i < 18; i += 2) { q2 = fmaf(xv[i] - mu, xv[i] - mu, q2); q3 = fmaf(xv[i + 1] - mu, xv[i + 1] - mu, q3); }
    const float rs = __builtin_amdgcn_rsqf(x16_sum(row_sum(q2 + q3)) * (1.f / D) + 1e-5f);
    const float m0 = rl(mu, 0), m1 = rl(mu, 32), r0 = rl(rs, 0), r1 = rl(rs, 32);
    const float m_ = bb ? m1 : m0, r_ = bb ? r1 : r0;
#pragma unroll
    for (int i = 0; i < 9; ++i) ya[i] = pk((lo(ya[i]) - m_) * r_, (hi(ya[i]) - m_) * r_);
  }
  FENCE();
  f32x4 acc0 = {0.f, 0.f, 0.f, 0.f}, acc1 = acc0;
  sfor<0, NA>([&](auto i_) {
    constexpr int i = decltype(i_)::v;
    acc0 = mfma(diag(duplo(ya[i]), msk), wa[i], acc0);
    acc1 = mfma(diag(duphi(ya[i]), msk), wa[i], acc1);
    if constexpr (i >= NA - ILV) { FENCE(); issue(IC<i - (NA - ILV)>{}); FENCE(); }
  });
#pragma unroll
  for (int r = 0; r < 4; ++r) acc0[r] += dpp<ROR8>(acc1[r]);
  auto publish_a = [&](int nc) {
    const u32 vs = c < nc ? WS_A + (u32)(ha * 8 + ja) * 8192 + w * 2048 + lane * 16 : OOB;
    __builtin_amdgcn_raw_buffer_store_b128(u32x4{__float_as_uint(acc0[0]), tag, __float_as_uint(acc0[1]), tag}, rw, vs, 0, SC1);
    __builtin_amdgcn_raw_buffer_store_b128(u32x4{__float_as_uint(acc0[2]), tag, __float_as_uint(acc0[3]), tag}, rw, vs, 1024, SC1);
  };
  if (w < 2) {
    publish_a(8);
  } else {
    f32x4 e0 = {0.f, 0.f, 0.f, 0.f}, e1 = e0;
#pragma unroll
    for (int i = 0; i < 9; ++i) {
      const u32x4 z = {0u, 0u, 0u, 0u};
      e0 = mfma(diag(duplo(ya[i]), msk), c == i ? we[0] : z, e0);
      e1 = mfma(diag(duphi(ya[i]), msk), c == i ? we[1] : z, e1);
    }
#pragma unroll
    for (int r = 0; r < 4; ++r) { const float e = row_sum(e0[r] + e1[r]); acc0[r] = c == 8 ? e : acc0[r]; }
    publish_a(9);
  }
  FENCE();
  sfor<ILV, HPOS>(issue);
  FENCE();
  __builtin_amdgcn_raw_buffer_store_b64(u32x2{0u, 0u}, rw, head == 0 ? vsum : OOB, 0, SC1);
  FENCE();
  sfor<HPOS, N2>(issue);
  FENCE();
  const int slot = 5 * w + (lane & 7), sb = slot >= 9, kk = slot - 9 * sb;
  const bool hok = (lane & 7) < 5 && slot < 18;
  u32x4 qa, qb, hp;
  const u32 htag = hok ? tag : 0u;
  const u32 vqg = WS_A + (u32)(head * 8 + g) * 8192 + bb * 2048 + (w & 1) * 1024 + ((2 * b + (w >> 1)) * 16 + (c & 7)) * 16;
  u32 vhg;
  {
    const int u = 18 * j + 2 * kk, uw = u >= 72, uu = u - 72 * uw;
    vhg = hok ? WS_A + (u32)(head * 8 + (lane >> 3)) * 8192 + (2 + uw) * 2048 + ((uu >> 1) & 1) * 1024 + ((2 * sb + ((uu >> 2) & 1)) * 16 + (uu >> 3)) * 16 : OOB;
  }
  auto gather_a = [&]() { constexpr int X = SC1; qa = ld16<X>(rw, vqg, 0); qb = ld16<X>(rw, vqg, 4 * 8192); hp = ld16<X>(rw, vhg, 0); };
  gather_a();
  const u32 b1v = __builtin_amdgcn_raw_buffer_load_b32(mk(a.b1, F * 2), hok ? (u32)(me * 18 + 2 * kk) * 2 : OOB, 0, 0);
  const u32 b2v = ld2<0>(mk(a.b2, D * 2), (u32)(72 * j + to - 72 * (w >> 1)) * 2, 0);
  FENCE();
  constexpr int NG = (N2 / PN + 1) * PN;
  sfor<N2, NG>(issue);
  FENCE();
  float gate0, gate1;
  {
    float z = 0.f;
#pragma unroll
    for (int i = 0; i < 9; ++i) z = dot2(s_x[(lane >> 5) * 288 + (lane & 31) + 32 * i], s_x[576 + (lane & 31) + 32 * i], z);
    const float gt = 1.f / (1.f + __expf(-x16_sum(row_sum(z))));
    gate0 = rl(gt, 0); gate1 = rl(gt, 32);
    asm volatile("" : "+s"(gate0), "+s"(gate1));
  }
  FENCE();
  sfor<NG, N3>(issue);
  FENCE();
  if (any(bad2(qa, tag) | bad2(qb, tag) | bad2(hp, htag))) {
#pragma unroll 1
    for (u32 spin = 0; spin < SPIN_CAP; ++spin) {
      asm volatile("" ::: "memory");
      gather_a();
      if (!any(bad2(qa, tag) | bad2(qb, tag) | bad2(hp, htag))) break;
      __builtin_amdgcn_s_sleep(1);
    }
  }
  {
    const float q0 = x32_sum(x16_sum(__uint_as_float(qa[0]) + __uint_as_float(qb[0])));
    const float q1 = x32_sum(x16_sum(__uint_as_float(qa[2]) + __uint_as_float(qb[2])));
    if (lane < 16) s_qp[c * 4 + w] = pk(q0, q1);
  }
  wg_barrier();
  const u32x4 qp = *reinterpret_cast<const u32x4*>(&s_qp[c * 4]);
  FENCE();

  float sc[4];
  sfor<0, 4>([&](auto t_) {
    constexpr int t = decltype(t_)::v;
    float x[16];
    sfor<0, 16>([&](auto i_) {
      constexpr int i = decltype(i_)::v, n = 16 * t + i;
      float d = dot2(buf[n][0], qp[0], 0.f);
      d = dot2(buf[n][1], qp[1], d); d = dot2(buf[n][2], qp[2], d); x[i] = dot2(buf[n][3], qp[3], d);
      FENCE();
      sfor<issued(n), issued(n + 1)>(issue);
      FENCE();
      if constexpr (n == HPOS) {
        float h0 = __uint_as_float(hp[0]), h1 = __uint_as_float(hp[2]);
        h0 += dpp<ROR8>(h0); h1 += dpp<ROR8>(h1);
        h0 = gelu(x32_sum(x16_sum(h0)) + lo(b1v)); h1 = gelu(x32_sum(x16_sum(h1)) + hi(b1v));
        __builtin_amdgcn_raw_buffer_store_b64(u32x2{pk(h0, h1), tag}, rw, (hok && lane < 8) ? WS_H + (u32)head * 1152 + sb * 576 + (9 * j + kk) * 8 : OOB, 0, SC1);
        __builtin_amdgcn_raw_buffer_store_b64(u32x2{0u, tag}, rw, head == 0 && lane == 0 ? vflag : OOB, 0, SC1);
        FENCE();
      }
    });
#define TR_STEP(HALF, CTRL) _Pragma("unroll") for (int k = 0; k < (HALF); ++k) { \
      const float t1 = x[k] + dpp<CTRL>(x[k]), t2 = x[k + (HALF)] + dpp<CTRL>(x[k + (HALF)]); x[k] = (c & (HALF)) ? t2 : t1; }
    TR_STEP(8, ROR8) TR_STEP(4, XOR7) TR_STEP(2, XOR2) TR_STEP(1, XOR1)
#undef TR_STEP
    sc[t] = x[0] * (0.08838834764831845f * 1.4426950408889634f);
    FENCE();
  });
  u32 pp[4];
  const float mw = x32_max(x16_max(row_max(fmaxf(fmaxf(sc[0], sc[1]), fmaxf(sc[2], sc[3])))));
#pragma unroll
  for (int t = 0; t < 4; ++t) { sc[t] = __builtin_amdgcn_exp2f(sc[t] - mw); pp[t] = pk(sc[t], sc[t]); }
  FENCE();
  float scl;
  u32 hd[5];
  acc0 = f32x4{0.f, 0.f, 0.f, 0.f}; acc1 = acc0;
  f32x4 am = {0.f, 0.f, 0.f, 0.f};
  auto w2mac = [&](auto n_) {
    constexpr int n = decltype(n_)::v, i = w2idx(n);
    am = mfma(diag(i == 8 ? hd[4] : (i & 1) ? duphi(hd[i >> 1]) : duplo(hd[i >> 1]), msk), buf[n], am);
  };
  sfor<P_V, P_2>([&](auto n_) {
    constexpr int n = decltype(n_)::v, i = vidx(n);
    if constexpr (n == P_V + GPOS) {
      if (any(bad2(ml, tag) | bad2(h01, tag) | bad2(h23, tag) | (h4[1] ^ tag) | (fl[1] ^ tag))) {
#pragma unroll 1
        for (u32 spin = 0; spin < SPIN_CAP; ++spin) {
          asm volatile("" ::: "memory");
          gather_mlh();
          if (!any(bad2(ml, tag) | bad2(h01, tag) | bad2(h23, tag) | (h4[1] ^ tag) | (fl[1] ^ tag))) break;
          __builtin_amdgcn_s_sleep(1);
        }
      }
      const float ms = __uint_as_float(ml[0]), ls = __uint_as_float(ml[2]);
      const float mx = row_max(ms);
      scl = __builtin_amdgcn_exp2f(mw - mx) / row_sum(__builtin_amdgcn_exp2f(ms - mx) * ls);
      hd[0] = h01[0]; hd[1] = h01[2]; hd[2] = h23[0]; hd[3] = h23[2]; hd[4] = (g & 1) ? duphi(h4[0]) : duplo(h4[0]);
      asm volatile("" : "+v"(scl), "+v"(hd[4]));
      asm volatile("" : : "v"(pw0), "v"(pw1), "v"(pw2));
      FENCE();
    }
    if constexpr (w2idx(n) >= 0) {
      w2mac(n_);
    } else if constexpr (widx(n) < 0) {
      const u32x4 A = diag(dppu<BCAST + (i & 15)>(pp[i >> 4]), msk);
      if constexpr (i & 1) acc1 = mfma(A, buf[n], acc1); else acc0 = mfma(A, buf[n], acc0);
    }
    FENCE();
    sfor<issued(n), issued(n + 1)>(issue);
    FENCE();
    if constexpr (n == P_V + 3) {
      const float l = x32_sum(x16_sum(row_sum((sc[0] + sc[1]) + (sc[2] + sc[3]))));
      __builtin_amdgcn_raw_buffer_store_b128(u32x4{__float_as_uint(mw), tag, __float_as_uint(l), tag}, rw, lane == 0 ? WS_ML + (u32)me * 64 + w * 16 : OOB, 0, SC1);
      FENCE();
    }
  });
  if (lane < 32) *reinterpret_cast<f32x4*>(&s_o[w * 128 + 8 * c + 4 * g]) = scl * (acc0 + acc1);
  wg_barrier();
  static_assert(issued(P_2) == P_END, "the stream is out before the hand-off");
  {
    const int d0 = 32 * w + 8 * g + (c & 4);
    const bool own = bb * 4 + (c & 3) == j;
    const u32 vog = WS_O + (u32)(head * 8 + bb * 4 + (c & 3)) * 512 + (u32)d0 * 4;
    const f32x4 ov = ((*reinterpret_cast<const f32x4*>(&s_o[d0]) + *reinterpret_cast<const f32x4*>(&s_o[128 + d0])) +
                            (*reinterpret_cast<const f32x4*>(&s_o[256 + d0]) + *reinterpret_cast<const f32x4*>(&s_o[384 + d0])));
    u32 o01 = pk(ov[0], ov[1]), o23 = pk(ov[2], ov[3]);
    __builtin_amdgcn_raw_buffer_store_b128(u32x4{o01, tag, o23, tag}, rw, own ? vog : OOB, 0, SC1);
    FENCE();
    const u64 need = __builtin_amdgcn_ballot_w64(!own);
    poll_start(rw, vog, need);
    FENCE();
    const float gt = (g >> 1) ? gate1 : gate0;
    sfor<P_2, P_V + NV + NW2>(w2mac);
    am = (1.f - gt) * am;
    asm volatile("" : "+v"(am));
    FENCE();
    poll_wait(rw, vog, need, tag, o01, o23);
    float v[4] = {lo(o01), hi(o01), lo(o23), hi(o23)};
#pragma unroll
    for (int k = 0; k < 4; ++k) { v[k] += dpp<XOR1>(v[k]); v[k] += dpp<XOR2>(v[k]); }
    const u32 m0 = pk(v[0], v[1]), m1 = pk(v[2], v[3]), t0 = dppu<XOR7>(m0), t1 = dppu<XOR7>(m1);
    const u32 od[4] = {(c & 4) ? t0 : m0, (c & 4) ? t1 : m1, (c & 4) ? m0 : t0, (c & 4) ? m1 : t1};
    f32x4 aa = {0.f, 0.f, 0.f, 0.f}, aa1 = aa;
    sfor<0, 4>([&](auto i_) {
      constexpr int i = decltype(i_)::v;
      aa = mfma(diag(duplo(od[i]), msk), buf[wpos(NW2 + 2 * i)], aa);
      aa1 = mfma(diag(duphi(od[i]), msk), buf[wpos(NW2 + 2 * i + 1)], aa1);
    });
    if (c < 9) *reinterpret_cast<f32x4*>(&s_out[w * 144 + (g >> 1) * 72 + 8 * c + 4 * (g & 1)]) = gt * (aa + aa1) + am;
  }
  wg_barrier();
  {
    const float o = (s_out[to] + s_out[144 + to]) + (s_out[288 + to] + s_out[432 + to]);
    const bool fits = __builtin_fabsf(o) < 255.f;
    const float os = fits ? fmaf(o, 0x1p18f, 0x1p-16f) : 0.f, oh = __builtin_floorf(os);
    const u32 alo = ((u32)((os - oh) * 0x1p15f) << 17) + (!fits ? 33u * 64 + 1 : __builtin_fabsf(o) < 0x1p-15f ? 65u : 1u);
    const u32 ahi = (u32)((int)oh + (os - oh >= 1.f));
    u32x2 old = {alo, ahi};
    asm volatile("buffer_atomic_add_x2 %0, %1, %2, 0 offen sc0 sc1" : "+v"(old) : "v"(vsum), "s"(rw) : "memory");
    const u32 vraw = WS_R + (u32)j * (32 * 1152) + to * 8;
    __builtin_amdgcn_raw_buffer_store_b64(u32x2{__float_as_uint(o), tag}, rw, lane < 36 ? vraw + head * 1152 : OOB, 0, SC1);
    float bias = (1.f - ((w >> 1) ? gate1 : gate0)) * lo(b2v);
    uint16_t* po = a.out + (w >> 1) * (D - 72) + 72 * j + to;
    asm volatile("" : "+v"(bias), "+v"(po));
    asm volatile("s_waitcnt vmcnt(1)" : "+v"(old) : : "memory");
    if (lane < 36 && (old[0] & 63u) == 31u) {
      const u64 tot = __builtin_bit_cast(u64, old) + (((u64)ahi << 32) | alo);
      float r = fmaf((float)(int)(tot >> 32), 0x1p-18f, (float)((u32)tot >> 17) * 0x1p-33f) + bias;
      if (((u32)tot & 0x1f800u) != 0u) {
        r = bias;
#pragma unroll 1
        for (u32 h = 0, spin = 0; h < H && spin < SPIN_CAP; ++spin) {
          asm volatile("" ::: "memory");
          const u32x2 p = ld8<SC1>(rw, vraw + h * 1152, 0);
          if (p[1] == tag) { r += __uint_as_float(p[0]); ++h; }
        }
      }
      *(__attribute__((address_space(1))) uint16_t*)po = (uint16_t)pk(r, r);
      if (j == 0 && tid == 0) *reinterpret_cast<u32*>(a.ws + WS_NONCE) = nonce + 1u;
    }
  }
}

}

extern "C" void kernel_launch(void* const* d_in, const int* in_sizes, int n_in, void* d_out, int out_size,
                              void* d_ws, size_t ws_size, hipStream_t stream) {
  Args a;
  a.x = (const uint16_t*)d_in[0]; a.k = (const uint16_t*)d_in[1]; a.v = (const uint16_t*)d_in[2];
  a.wq = (const uint16_t*)d_in[3]; a.wo = (const uint16_t*)d_in[4];
  a.w1 = (const uint16_t*)d_in[6]; a.b1 = (const uint16_t*)d_in[7]; a.w2 = (const uint16_t*)d_in[8];
  a.b2 = (const uint16_t*)d_in[9]; a.gw = (const uint16_t*)d_in[10];
  a.out = (uint16_t*)d_out; a.ws = (unsigned char*)d_ws;
  hipLaunchKernelGGL(fused_decode, dim3(NWG), dim3(NTHR), 0, stream, a);
}
```

```cpp
#include <hip/hip_runtime.h>
#include <hip/hip_bf16.h>
#include <stdint.h>

namespace {

typedef unsigned u32;
typedef unsigned long long u64;
typedef __attribute__((ext_vector_type(2))) unsigned u32x2;
typedef __attribute__((ext_vector_type(4))) unsigned u32x4;
typedef __attribute__((ext_vector_type(4))) float f32x4;
typedef __attribute__((ext_vector_type(8))) __bf16 bf16x8;
typedef __attribute__((ext_vector_type(2))) __bf16 bf16x2;
typedef __amdgpu_buffer_rsrc_t rsrc_t;

constexpr int D = 576, H = 32, SK = 4096, DH = 128, F = 4608;
constexpr int NWG = 256, NTHR = 256;
constexpr int WIN = 12;
constexpr int ILV = 8;
constexpr int N2 = 20;
constexpr int N3 = 32;
constexpr int HPOS = 10;
constexpr int GPOS = 36;
constexpr int W2POS = 44;
constexpr int NW2E = 11;
constexpr int WSP = 1;
constexpr int PACE = 141;
constexpr int W2B = 780;
constexpr int POFF = 600;
constexpr int PN = 2;
constexpr int LA3 = 6;
constexpr int EC = 38;
constexpr int RB = 120;
constexpr int CPF = 18496;
constexpr int SB = 640;
constexpr int SS = 128;
constexpr u32 OOB = 0x80000000u;
constexpr int SC1 = 16, NT = 2;

constexpr u32 WS_NONCE = 0;
constexpr u32 WS_ML = 4096;
constexpr u32 WS_H = WS_ML + NWG * 64;
constexpr u32 WS_O = WS_H + H * 1152;
constexpr u32 WS_R = WS_O + NWG * 512;
constexpr u32 WS_S = WS_R + 8 * 18 * 32 * 64;
constexpr u32 WS_F = WS_S + 32 * SB;
constexpr u32 WS_A = WS_F + 8 * 128;
constexpr u32 WS_END = WS_A + NWG * 8192;
constexpr u32 SPIN_CAP = 1u << 18;

__device__ __forceinline__ rsrc_t mk(const void* p, u32 bytes) {
  return __builtin_amdgcn_make_buffer_rsrc(const_cast<void*>(p), 0, bytes, 0x00020000);
}
template <int AUX> __device__ __forceinline__ u32x4 ld16(rsrc_t r, u32 v, u32 s) { return __builtin_amdgcn_raw_buffer_load_b128(r, v, s, AUX); }
template <int AUX> __device__ __forceinline__ u32x2 ld8(rsrc_t r, u32 v, u32 s) { return __builtin_amdgcn_raw_buffer_load_b64(r, v, s, AUX); }
template <int AUX> __device__ __forceinline__ u32 ld2(rsrc_t r, u32 v, u32 s) { return (u32)__builtin_amdgcn_raw_buffer_load_b16(r, v, s, AUX); }
__device__ __forceinline__ float lo(u32 x) { return __builtin_bit_cast(float, x << 16); }
__device__ __forceinline__ float hi(u32 x) { return __builtin_bit_cast(float, x & 0xffff0000u); }
__device__ __forceinline__ float dot2(u32 a, u32 b, float c) {
  return __builtin_amdgcn_fdot2_f32_bf16(__builtin_bit_cast(bf16x2, a), __builtin_bit_cast(bf16x2, b), c, false);
}
__device__ __forceinline__ u32 pk(float a, float b) {
  bf16x2 r = {(__bf16)a, (__bf16)b};
  return __builtin_bit_cast(u32, r);
}
template <int CTRL> __device__ __forceinline__ float dpp(float x) {
  return __builtin_bit_cast(float, __builtin_amdgcn_mov_dpp(__builtin_bit_cast(int, x), CTRL, 0xf, 0xf, true));
}
template <int CTRL> __device__ __forceinline__ u32 dppu(u32 x) { return (u32)__builtin_amdgcn_mov_dpp((int)x, CTRL, 0xf, 0xf, true); }
constexpr int XOR1 = 0xB1, XOR2 = 0x4E, XOR7 = 0x141, ROR8 = 0x128, BCAST = 0x150;
__device__ __forceinline__ float x16_sum(float x) {
  auto s = __builtin_amdgcn_permlane16_swap(__float_as_uint(x), __float_as_uint(x), false, false);
  return __uint_as_float(s[0]) + __uint_as_float(s[1]);
}
__device__ __forceinline__ float x32_sum(float x) {
  auto s = __builtin_amdgcn_permlane32_swap(__float_as_uint(x), __float_as_uint(x), false, false);
  return __uint_as_float(s[0]) + __uint_as_float(s[1]);
}
__device__ __forceinline__ float x16_max(float x) {
  auto s = __builtin_amdgcn_permlane16_swap(__float_as_uint(x), __float_as_uint(x), false, false);
  return fmaxf(__uint_as_float(s[0]), __uint_as_float(s[1]));
}
__device__ __forceinline__ float x32_max(float x) {
  auto s = __builtin_amdgcn_permlane32_swap(__float_as_uint(x), __float_as_uint(x), false, false);
  return fmaxf(__uint_as_float(s[0]), __uint_as_float(s[1]));
}
__device__ __forceinline__ float row_sum(float x) { x += dpp<XOR1>(x); x += dpp<XOR2>(x); x += dpp<XOR7>(x); x += dpp<ROR8>(x); return x; }
__device__ __forceinline__ float row_max(float x) { x = fmaxf(x, dpp<XOR1>(x)); x = fmaxf(x, dpp<XOR2>(x)); x = fmaxf(x, dpp<XOR7>(x)); x = fmaxf(x, dpp<ROR8>(x)); return x; }
__device__ __forceinline__ f32x4 mfma(u32x4 a, u32x4 b, f32x4 c) {
  return __builtin_amdgcn_mfma_f32_16x16x32_bf16(__builtin_bit_cast(bf16x8, a), __builtin_bit_cast(bf16x8, b), c, 0, 0, 0);
}
__device__ __forceinline__ u32x4 diag(u32 dup, u32x4 m) { return u32x4{dup & m[0], dup & m[1], dup & m[2], dup & m[3]}; }
__device__ __forceinline__ u32 duplo(u32 x) { return __builtin_amdgcn_perm(x, x, 0x01000100u); }
__device__ __forceinline__ u32 duphi(u32 x) { return __builtin_amdgcn_perm(x, x, 0x03020302u); }
__device__ __forceinline__ void wg_barrier() {
  asm volatile("s_waitcnt lgkmcnt(0)\n\ts_barrier" ::: "memory");
}
template <int T> __device__ __forceinline__ void pace(u32 t0) {
  asm volatile("1:\n\ts_memrealtime vcc\n\ts_waitcnt lgkmcnt(0)\n\ts_sub_u32 vcc_lo, vcc_lo, %0\n\ts_cmpk_lt_u32 vcc_lo, %1\n\ts_cbranch_scc1 1b"
               : : "s"(t0), "n"(T) : "vcc", "scc");
}
#define FENCE() __builtin_amdgcn_sched_barrier(0)
template <int N> struct IC { static constexpr int v = N; };
template <int B, int E, class Fn> __device__ __forceinline__ void sfor(Fn&& f) {
  if constexpr (B < E) { f(IC<B>{}); sfor<B + 1, E>(f); }
}

struct Args {
  const uint16_t* x; const uint16_t* k; const uint16_t* v; const uint16_t* wq; const uint16_t* wo;
  const uint16_t* w1; const uint16_t* b1; const uint16_t* w2; const uint16_t* b2; const uint16_t* gw;
  uint16_t* out; unsigned char* ws;
};

constexpr int NA = 9, NK = 64, NV = 64, NW2 = 9, NWO = 8;
constexpr int P_V = NK, P_2 = P_V + NV + NW2E, P_END = P_V + NV + NW2 + NWO;
constexpr int wpos(int e) { return e < NW2E ? P_V + W2POS + e * (WSP + 1) : P_V + NV + e; }
constexpr int widx(int n) { for (int e = 0; e < NW2 + NWO; ++e) if (wpos(e) == n) return e; return -1; }
constexpr int w2idx(int n) { return widx(n) < NW2 ? widx(n) : -1; }
constexpr int vidx(int n) { int v = n - P_V; for (int e = 0; e < NW2E; ++e) v -= wpos(e) < n; return v; }
static_assert(W2POS > GPOS && W2POS + (NW2E - 1) * WSP < NV, "early weight loads sit inside V");
constexpr u32 chunk(int n) { return ((u32)(n >> 1) << 15) + ((u32)(n & 1) << 12) + ((u32)((n >> 1) & 3) << 13); }
constexpr int due(int n) {
  int bytes = 72 * 272 * 2 / 4;
  for (int i = 0; i < n; ++i) bytes += widx(i) >= 0 ? W2B : 1024;
  return (POFF + bytes * PACE / 1024) / 10;
}
constexpr int imax(int a, int b) { return a > b ? a : b; }
constexpr int imin(int a, int b) { return a < b ? a : b; }
constexpr int issued(int n) { return imin(P_END, imax(N3 + n / 3, n + WIN)); }
constexpr bool paced(int n) { return n % PN == 0 && n >= ILV && n < P_END - LA3; }
__device__ __forceinline__ u32 bad2(u32x4 v, u32 tag) { return (v[1] ^ tag) | (v[3] ^ tag); }
__device__ __forceinline__ bool any(u32 bad) { return __builtin_amdgcn_ballot_w64(bad != 0) != 0; }
#define P_REGS "v224", "v225", "v226", "v227"
#define P_LD "buffer_load_dwordx4 v[224:227], %[v], %[rw], 0 offen sc1\n\t"
__device__ __forceinline__ void poll_start(rsrc_t rw, u32 v, u64 need) {
  asm volatile("s_nop 4\n\ts_mov_b64 exec, %[need]\n\t" P_LD "s_mov_b64 exec, -1"
      : : [v] "v"(v), [rw] "s"(rw), [need] "s"(need) : "memory", P_REGS);
}
__device__ __forceinline__ void poll_wait(rsrc_t rw, u32 v, u64 need, u32 tag, u32& x, u32& y) {
  u64 t; u32 cnt = SPIN_CAP;
  asm volatile("s_nop 4\n\ts_mov_b64 exec, %[need]\n\t"
      "1:\n\ts_waitcnt vmcnt(0)\n\t"
      "v_cmp_eq_u32_e32 vcc, %[tag], v225\n\tv_cmp_eq_u32_e64 %[t], %[tag], v227\n\ts_and_b64 exec, vcc, %[t]\n\t"
      "v_mov_b32_e32 %[x], v224\n\tv_mov_b32_e32 %[y], v226\n\t"
      "s_andn2_b64 %[need], %[need], exec\n\ts_mov_b64 exec, %[need]\n\ts_cbranch_scc0 2f\n\t" P_LD
      "s_sub_u32 %[cnt], %[cnt], 1\n\ts_cbranch_scc0 1b\n\t"
      "s_waitcnt vmcnt(0)\n\t"
      "2:\n\ts_mov_b64 exec, -1\n\ts_nop 1"
      : [x] "+v"(x), [y] "+v"(y), [need] "+s"(need), [cnt] "+s"(cnt), [t] "=&s"(t)
      : [v] "v"(v), [rw] "s"(rw), [tag] "s"(tag) : "memory", "vcc", "scc", P_REGS);
}
__device__ __forceinline__ float gelu(float v) {
  const float u = 1.5957691216057308f * (v + 0.044715f * v * v * v);
  return v / (1.f + __expf(-u));
}

__global__ __launch_bounds__(NTHR, 2) void fused_decode(Args a) {
  __shared__ __attribute__((aligned(16))) u32 s_x[864];
  __shared__ __attribute__((aligned(16))) u32 s_qp[64];
  __shared__ __attribute__((aligned(16))) float s_o[4 * 128];
  __shared__ __attribute__((aligned(16))) float s_out[4 * 144];

  const int tid = threadIdx.x, lane = tid & 63, g = lane >> 4, c = lane & 15, bb = c >> 3;
  const int w = __builtin_amdgcn_readfirstlane(tid >> 6);
  const int bid = blockIdx.x, head = (bid & 7) * 4 + (bid >> 6), j = (bid >> 3) & 7, b = j >> 2, sp = j & 3;
  const int me = head * 8 + j;
  const int ha = bid >> 3, ja = bid & 7;
  const u64 entry = __builtin_amdgcn_s_memrealtime();
  u32 t0;

  if (w == 1) {
    const char* pc = reinterpret_cast<const char*>(__builtin_amdgcn_s_getpc() & ~255ull);
    const u32 off = (u32)((bid >> 3) + 32 * lane) * 64u;
    if (off < CPF) asm volatile("global_load_dword v228, %0, %1" : : "v"(off), "s"(pc) : "v228");
  }
  u32 nonce = *(const __attribute__((address_space(4))) u32*)(a.ws + WS_NONCE);
  const int xid = 54 * w + (lane < 54 ? lane : 53);
  const u32x4 xin = *(xid < 144 ? reinterpret_cast<const u32x4*>(a.x) + xid : reinterpret_cast<const u32x4*>(a.gw) + (xid - 144));
  FENCE();
  u32x4 wa[NA], we[2];
  {
    const bool isq = w < 2;
    const u32 stride = isq ? H * DH * 2 : F * 2;
    const rsrc_t ra = mk(isq ? a.wq : a.w1, D * stride);
    const u32 col = isq ? ha * 256 + w * 128 : ha * 288 + (w - 2) * 144;
    const u32 va = (u32)(72 * ja + 18 * g + bb) * stride + col + (c & 7) * 16;
#pragma unroll
    for (int i = 0; i < NA; ++i) wa[i] = ld16<NT>(ra, va, 2 * i * stride);
    const u32 ve = c < 9 && !isq ? (u32)(72 * ja + 18 * g + 2 * c) * stride + col + 128 : OOB;
    we[0] = ld16<NT>(ra, ve, 0); we[1] = ld16<NT>(ra, ve, stride);
  }
  FENCE();

  const size_t kvoff = (size_t)(b * H + head) * SK * DH;
  const rsrc_t rk = mk(a.k + kvoff, SK * DH * 2), rv = mk(a.v + kvoff, SK * DH * 2);
  const rsrc_t ro = mk(a.wo + (size_t)head * DH * D, DH * D * 2);
  const rsrc_t r2 = mk(a.w2 + (size_t)head * 144 * D, 144 * D * 2);
  const rsrc_t rw = mk(a.ws, WS_END);
  const u32 vkv = (u32)tid * 16, skv = (u32)sp << 13;
  const u32 vcol = c < 9 ? (u32)j * 144 + c * 16 : OOB;
  const u32 vo = vcol + (u32)(32 * w + 8 * g) * (D * 2), v2 = vcol + (u32)(36 * w + 8 * g) * (D * 2), v2t = vcol + (u32)(36 * w + 32 + g) * (D * 2);
  const u32 vml = WS_ML + (u32)(head * 8 + b * 4) * 64 + c * 16;
  const u32 vh = WS_H + (u32)head * 1152 + bb * 576 + (18 * w + 4 * g) * 8, vht = WS_H + (u32)head * 1152 + bb * 576 + (18 * w + 16 + (g >> 1)) * 8;

  const int to = 36 * w + (lane < 36 ? lane : 0);
  const u32 vsum = lane < 36 ? WS_S + (u32)(j * 4 + w) * SB + (lane >> 3) * SS + (lane & 7) * 8 : OOB, vflag = WS_F + (u32)j * 128 + w * 8;
  u32x4 buf[P_END], ml, h01, h23; u32x2 h4, fl; u32 pw0, pw1, pw2;
  auto gather_mlh = [&]() { constexpr int X = SC1; ml = ld16<X>(rw, vml, 0); h01 = ld16<X>(rw, vh, 0); h23 = ld16<X>(rw, vh, 16); h4 = ld8<X>(rw, vht, 0); fl = ld8<X>(rw, vflag, 0); };
  auto issue = [&](auto n_) {
    constexpr int n = decltype(n_)::v;
    if constexpr (paced(n)) pace<due(n)>(t0);
    if constexpr (n == P_V + GPOS) {
      pw0 = __builtin_amdgcn_raw_buffer_load_b32(rw, lane == 0 ? WS_O + (u32)me * 512 + w * 128 : OOB, 0, SC1);
      pw1 = __builtin_amdgcn_raw_buffer_load_b32(mk(a.out, 2 * D * 2), tid == 0 && head < 5 ? (u32)(j * 5 + head) * 64 : OOB, 0, SC1);
      pw2 = __builtin_amdgcn_raw_buffer_load_b32(rw, lane < 3 ? WS_R + (u32)j * (32 * 1152) + head * 1152 + (3 * w + lane) * 96 : OOB, 0, SC1);
      gather_mlh();
    }
    if constexpr (n < P_V) buf[n] = ld16<NT>(rk, vkv, skv ^ chunk(n));
    else if constexpr (w2idx(n) == NW2 - 1) buf[n] = ld16<NT>(r2, v2t, 0);
    else if constexpr (w2idx(n) >= 0) buf[n] = ld16<NT>(r2, v2, w2idx(n) * (D * 2));
    else if constexpr (widx(n) >= 0) buf[n] = ld16<NT>(ro, vo, (widx(n) - NW2) * (D * 2));
    else buf[n] = ld16<NT>(rv, vkv, skv ^ chunk(vidx(n)));
  };

  asm volatile("" : "+s"(nonce));
  asm volatile("" : : "s"(entry));
  const u32 tag = ((nonce + 1u) * 0x9E3779B1u) ^ 0x7F4A7C15u;
  asm volatile("" ::"s"(tag));

  if (lane < 54) *reinterpret_cast<u32x4*>(&s_x[xid * 4]) = xin;
  wg_barrier();
  FENCE();
  {
    const u32 now = (u32)__builtin_amdgcn_s_memrealtime();
    const u32 late = now - (u32)entry;
    t0 = late > 2 * (RB - EC) ? now - RB : (u32)entry + (late >> 1) - EC;
    asm volatile("" : "+s"(t0));
  }
  FENCE();

  u32x4 msk;
#pragma unroll
  for (int r = 0; r < 4; ++r) msk[r] = (c & 7) == 2 * r ? 0x0000ffffu : (c & 7) == 2 * r + 1 ? 0xffff0000u : 0u;

  auto rl = [](float v, int l) { return __builtin_bit_cast(float, __builtin_amdgcn_readlane(__builtin_bit_cast(int, v), l)); };
  u32 ya[9];
#pragma unroll
  for (int i = 0; i < 9; ++i) ya[i] = s_x[bb * 288 + 36 * ja + 9 * g + i];
  if (w >= 2) {
    const int o = (lane >> 5) * 288 + (lane & 31);
    float xv[18], s = 0.f;
#pragma unroll
    for (int i = 0; i < 9; ++i) {
      const u32 xp = s_x[o + 32 * i];
      xv[2 * i] = lo(xp); xv[2 * i + 1] = hi(xp);
      s += xv[2 * i] + xv[2 * i + 1];
    }
    const float mu = x16_sum(row_sum(s)) * (1.f / D);
    float q2 = 0.f, q3 = 0.f;
#pragma unroll
    for (int i = 0; i < 18; i += 2) { q2 = fmaf(xv[i] - mu, xv[i] - mu, q2); q3 = fmaf(xv[i + 1] - mu, xv[i + 1] - mu, q3); }
    const float rs = __builtin_amdgcn_rsqf(x16_sum(row_sum(q2 + q3)) * (1.f / D) + 1e-5f);
    const float m0 = rl(mu, 0), m1 = rl(mu, 32), r0 = rl(rs, 0), r1 = rl(rs, 32);
    const float m_ = bb ? m1 : m0, r_ = bb ? r1 : r0;
#pragma unroll
    for (int i = 0; i < 9; ++i) ya[i] = pk((lo(ya[i]) - m_) * r_, (hi(ya[i]) - m_) * r_);
  }
  FENCE();
  f32x4 acc0 = {0.f, 0.f, 0.f, 0.f}, acc1 = acc0;
  sfor<0, NA>([&](auto i_) {
    constexpr int i = decltype(i_)::v;
    acc0 = mfma(diag(duplo(ya[i]), msk), wa[i], acc0);
    acc1 = mfma(diag(duphi(ya[i]), msk), wa[i], acc1);
    if constexpr (i >= NA - ILV) { FENCE(); issue(IC<i - (NA - ILV)>{}); FENCE(); }
  });
#pragma unroll
  for (int r = 0; r < 4; ++r) acc0[r] += dpp<ROR8>(acc1[r]);
  auto publish_a = [&](int nc) {
    const u32 vs = c < nc ? WS_A + (u32)(ha * 8 + ja) * 8192 + w * 2048 + lane * 16 : OOB;
    __builtin_amdgcn_raw_buffer_store_b128(u32x4{__float_as_uint(acc0[0]), tag, __float_as_uint(acc0[1]), tag}, rw, vs, 0, SC1);
    __builtin_amdgcn_raw_buffer_store_b128(u32x4{__float_as_uint(acc0[2]), tag, __float_as_uint(acc0[3]), tag}, rw, vs, 1024, SC1);
  };
  if (w < 2) {
    publish_a(8);
  } else {
    f32x4 e0 = {0.f, 0.f, 0.f, 0.f}, e1 = e0;
#pragma unroll
    for (int i = 0; i < 9; ++i) {
      const u32x4 z = {0u, 0u, 0u, 0u};
      e0 = mfma(diag(duplo(ya[i]), msk), c == i ? we[0] : z, e0);
      e1 = mfma(diag(duphi(ya[i]), msk), c == i ? we[1] : z, e1);
    }
#pragma unroll
    for (int r = 0; r < 4; ++r) { const float e = row_sum(e0[r] + e1[r]); acc0[r] = c == 8 ? e : acc0[r]; }
    publish_a(9);
  }
  FENCE();
  sfor<ILV, HPOS>(issue);
  FENCE();
  __builtin_amdgcn_raw_buffer_store_b64(u32x2{0u, 0u}, rw, head == 0 ? vsum : OOB, 0, SC1);
  FENCE();
  sfor<HPOS, N2>(issue);
  FENCE();
  const int slot = 5 * w + (lane & 7), sb = slot >= 9, kk = slot - 9 * sb;
  const bool hok = (lane & 7) < 5 && slot < 18;
  u32x4 qa, qb, hp;
  const u32 htag = hok ? tag : 0u;
  const u32 vqg = WS_A + (u32)(head * 8 + g) * 8192 + bb * 2048 + (w & 1) * 1024 + ((2 * b + (w >> 1)) * 16 + (c & 7)) * 16;
  u32 vhg;
  {
    const int u = 18 * j + 2 * kk, uw = u >= 72, uu = u - 72 * uw;
    vhg = hok ? WS_A + (u32)(head * 8 + (lane >> 3)) * 8192 + (2 + uw) * 2048 + ((uu >> 1) & 1) * 1024 + ((2 * sb + ((uu >> 2) & 1)) * 16 + (uu >> 3)) * 16 : OOB;
  }
  auto gather_a = [&]() { constexpr int X = SC1; qa = ld16<X>(rw, vqg, 0); qb = ld16<X>(rw, vqg, 4 * 8192); hp = ld16<X>(rw, vhg, 0); };
  gather_a();
  const u32 b1v = __builtin_amdgcn_raw_buffer_load_b32(mk(a.b1, F * 2), hok ? (u32)(me * 18 + 2 * kk) * 2 : OOB, 0, 0);
  const u32 b2v = ld2<0>(mk(a.b2, D * 2), (u32)(72 * j + to - 72 * (w >> 1)) * 2, 0);
  FENCE();
  constexpr int NG = (N2 / PN + 1) * PN;
  sfor<N2, NG>(issue);
  FENCE();
  float gate0, gate1;
  {
    float z = 0.f;
#pragma unroll
    for (int i = 0; i < 9; ++i) z = dot2(s_x[(lane >> 5) * 288 + (lane & 31) + 32 * i], s_x[576 + (lane & 31) + 32 * i], z);
    const float gt = 1.f / (1.f + __expf(-x16_sum(row_sum(z))));
    gate0 = rl(gt, 0); gate1 = rl(gt, 32);
    asm volatile("" : "+s"(gate0), "+s"(gate1));
  }
  FENCE();
  sfor<NG, N3>(issue);
  FENCE();
  if (any(bad2(qa, tag) | bad2(qb, tag) | bad2(hp, htag))) {
#pragma unroll 1
    for (u32 spin = 0; spin < SPIN_CAP; ++spin) {
      asm volatile("" ::: "memory");
      gather_a();
      if (!any(bad2(qa, tag) | bad2(qb, tag) | bad2(hp, htag))) break;
      __builtin_amdgcn_s_sleep(1);
    }
  }
  {
    const float q0 = x32_sum(x16_sum(__uint_as_float(qa[0]) + __uint_as_float(qb[0])));
    const float q1 = x32_sum(x16_sum(__uint_as_float(qa[2]) + __uint_as_float(qb[2])));
    if (lane < 16) s_qp[c * 4 + w] = pk(q0, q1);
  }
  wg_barrier();
  const u32x4 qp = *reinterpret_cast<const u32x4*>(&s_qp[c * 4]);
  FENCE();

  float sc[4];
  sfor<0, 4>([&](auto t_) {
    constexpr int t = decltype(t_)::v;
    float x[16];
    sfor<0, 16>([&](auto i_) {
      constexpr int i = decltype(i_)::v, n = 16 * t + i;
      float d = dot2(buf[n][0], qp[0], 0.f);
      d = dot2(buf[n][1], qp[1], d); d = dot2(buf[n][2], qp[2], d); x[i] = dot2(buf[n][3], qp[3], d);
      FENCE();
      sfor<issued(n), issued(n + 1)>(issue);
      FENCE();
      if constexpr (n == HPOS) {
        float h0 = __uint_as_float(hp[0]), h1 = __uint_as_float(hp[2]);
        h0 += dpp<ROR8>(h0); h1 += dpp<ROR8>(h1);
        h0 = gelu(x32_sum(x16_sum(h0)) + lo(b1v)); h1 = gelu(x32_sum(x16_sum(h1)) + hi(b1v));
        __builtin_amdgcn_raw_buffer_store_b64(u32x2{pk(h0, h1), tag}, rw, (hok && lane < 8) ? WS_H + (u32)head * 1152 + sb * 576 + (9 * j + kk) * 8 : OOB, 0, SC1);
        __builtin_amdgcn_raw_buffer_store_b64(u32x2{0u, tag}, rw, head == 0 && lane == 0 ? vflag : OOB, 0, SC1);
        FENCE();
      }
    });
#define TR_STEP(HALF, CTRL) _Pragma("unroll") for (int k = 0; k < (HALF); ++k) { \
      const float t1 = x[k] + dpp<CTRL>(x[k]), t2 = x[k + (HALF)] + dpp<CTRL>(x[k + (HALF)]); x[k] = (c & (HALF)) ? t2 : t1; }
    TR_STEP(8, ROR8) TR_STEP(4, XOR7) TR_STEP(2, XOR2) TR_STEP(1, XOR1)
#undef TR_STEP
    sc[t] = x[0] * (0.08838834764831845f * 1.4426950408889634f);
    FENCE();
  });
  u32 pp[4];
  const float mw = x32_max(x16_max(row_max(fmaxf(fmaxf(sc[0], sc[1]), fmaxf(sc[2], sc[3])))));
#pragma unroll
  for (int t = 0; t < 4; ++t) { sc[t] = __builtin_amdgcn_exp2f(sc[t] - mw); pp[t] = pk(sc[t], sc[t]); }
  FENCE();
  float scl;
  u32 hd[5];
  acc0 = f32x4{0.f, 0.f, 0.f, 0.f}; acc1 = acc0;
  f32x4 am = {0.f, 0.f, 0.f, 0.f};
  auto w2mac = [&](auto n_) {
    constexpr int n = decltype(n_)::v, i = w2idx(n);
    am = mfma(diag(i == 8 ? hd[4] : (i & 1) ? duphi(hd[i >> 1]) : duplo(hd[i >> 1]), msk), buf[n], am);
  };
  sfor<P_V, P_2>([&](auto n_) {
    constexpr int n = decltype(n_)::v, i = vidx(n);
    if constexpr (n == P_V + GPOS) {
      if (any(bad2(ml, tag) | bad2(h01, tag) | bad2(h23, tag) | (h4[1] ^ tag) | (fl[1] ^ tag))) {
#pragma unroll 1
        for (u32 spin = 0; spin < SPIN_CAP; ++spin) {
          asm volatile("" ::: "memory");
          gather_mlh();
          if (!any(bad2(ml, tag) | bad2(h01, tag) | bad2(h23, tag) | (h4[1] ^ tag) | (fl[1] ^ tag))) break;
          __builtin_amdgcn_s_sleep(1);
        }
      }
      const float ms = __uint_as_float(ml[0]), ls = __uint_as_float(ml[2]);
      const float mx = row_max(ms);
      scl = __builtin_amdgcn_exp2f(mw - mx) / row_sum(__builtin_amdgcn_exp2f(ms - mx) * ls);
      hd[0] = h01[0]; hd[1] = h01[2]; hd[2] = h23[0]; hd[3] = h23[2]; hd[4] = (g & 1) ? duphi(h4[0]) : duplo(h4[0]);
      asm volatile("" : "+v"(scl), "+v"(hd[4]));
      asm volatile("" : : "v"(pw0), "v"(pw1), "v"(pw2));
      FENCE();
    }
    if constexpr (w2idx(n) >= 0) {
      w2mac(n_);
    } else if constexpr (widx(n) < 0) {
      const u32x4 A = diag(dppu<BCAST + (i & 15)>(pp[i >> 4]), msk);
      if constexpr (i & 1) acc1 = mfma(A, buf[n], acc1); else acc0 = mfma(A, buf[n], acc0);
    }
    FENCE();
    sfor<issued(n), issued(n + 1)>(issue);
    FENCE();
    if constexpr (n == P_V + 3) {
      const float l = x32_sum(x16_sum(row_sum((sc[0] + sc[1]) + (sc[2] + sc[3]))));
      __builtin_amdgcn_raw_buffer_store_b128(u32x4{__float_as_uint(mw), tag, __float_as_uint(l), tag}, rw, lane == 0 ? WS_ML + (u32)me * 64 + w * 16 : OOB, 0, SC1);
      FENCE();
    }
  });
  if (lane < 32) *reinterpret_cast<f32x4*>(&s_o[w * 128 + 8 * c + 4 * g]) = scl * (acc0 + acc1);
  wg_barrier();
  static_assert(issued(P_2) == P_END, "the stream is out before the hand-off");
  {
    const int d0 = 32 * w + 8 * g + (c & 4);
    const bool own = bb * 4 + (c & 3) == j;
    const u32 vog = WS_O + (u32)(head * 8 + bb * 4 + (c & 3)) * 512 + (u32)d0 * 4;
    const f32x4 ov = ((*reinterpret_cast<const f32x4*>(&s_o[d0]) + *reinterpret_cast<const f32x4*>(&s_o[128 + d0])) +
                            (*reinterpret_cast<const f32x4*>(&s_o[256 + d0]) + *reinterpret_cast<const f32x4*>(&s_o[384 + d0])));
    u32 o01 = pk(ov[0], ov[1]), o23 = pk(ov[2], ov[3]);
    __builtin_amdgcn_raw_buffer_store_b128(u32x4{o01, tag, o23, tag}, rw, own ? vog : OOB, 0, SC1);
    FENCE();
    const u64 need = __builtin_amdgcn_ballot_w64(!own);
    poll_start(rw, vog, need);
    FENCE();
    const float gt = (g >> 1) ? gate1 : gate0;
    sfor<P_2, P_V + NV + NW2>(w2mac);
    am = (1.f - gt) * am;
    asm volatile("" : "+v"(am));
    FENCE();
    poll_wait(rw, vog, need, tag, o01, o23);
    float v[4] = {lo(o01), hi(o01), lo(o23), hi(o23)};
#pragma unroll
    for (int k = 0; k < 4; ++k) { v[k] += dpp<XOR1>(v[k]); v[k] += dpp<XOR2>(v[k]); }
    const u32 m0 = pk(v[0], v[1]), m1 = pk(v[2], v[3]), t0 = dppu<XOR7>(m0), t1 = dppu<XOR7>(m1);
    const u32 od[4] = {(c & 4) ? t0 : m0, (c & 4) ? t1 : m1, (c & 4) ? m0 : t0, (c & 4) ? m1 : t1};
    f32x4 aa = {0.f, 0.f, 0.f, 0.f}, aa1 = aa;
    sfor<0, 4>([&](auto i_) {
      constexpr int i = decltype(i_)::v;
      aa = mfma(diag(duplo(od[i]), msk), buf[wpos(NW2 + 2 * i)], aa);
      aa1 = mfma(diag(duphi(od[i]), msk), buf[wpos(NW2 + 2 * i + 1)], aa1);
    });
    if (c < 9) *reinterpret_cast<f32x4*>(&s_out[w * 144 + (g >> 1) * 72 + 8 * c + 4 * (g & 1)]) = gt * (aa + aa1) + am;
  }
  wg_barrier();
  {
    const float o = (s_out[to] + s_out[144 + to]) + (s_out[288 + to] + s_out[432 + to]);
    const bool fits = __builtin_fabsf(o) < 255.f;
    const float os = fits ? fmaf(o, 0x1p18f, 0x1p-16f) : 0.f, oh = __builtin_floorf(os);
    const u32 alo = ((u32)((os - oh) * 0x1p15f) << 17) + (!fits ? 33u * 64 + 1 : __builtin_fabsf(o) < 0x1p-15f ? 65u : 1u);
    const u32 ahi = (u32)((int)oh + (os - oh >= 1.f));
    u32x2 old = {alo, ahi};
    asm volatile("buffer_atomic_add_x2 %0, %1, %2, 0 offen sc0 sc1" : "+v"(old) : "v"(vsum), "s"(rw) : "memory");
    const u32 vraw = WS_R + (u32)j * (32 * 1152) + to * 8;
    __builtin_amdgcn_raw_buffer_store_b64(u32x2{__float_as_uint(o), tag}, rw, lane < 36 ? vraw + head * 1152 : OOB, 0, SC1);
    float bias = (1.f - ((w >> 1) ? gate1 : gate0)) * lo(b2v);
    uint16_t* po = a.out + (w >> 1) * (D - 72) + 72 * j + to;
    asm volatile("" : "+v"(bias), "+v"(po));
    asm volatile("s_waitcnt vmcnt(1)" : "+v"(old) : : "memory");
    if (lane < 36 && (old[0] & 63u) == 31u) {
      const u64 tot = __builtin_bit_cast(u64, old) + (((u64)ahi << 32) | alo);
      float r = fmaf((float)(int)(tot >> 32), 0x1p-18f, (float)((u32)tot >> 17) * 0x1p-33f) + bias;
      if (((u32)tot & 0x1f800u) != 0u) {
        r = bias;
#pragma unroll 1
        for (u32 h = 0, spin = 0; h < H && spin < SPIN_CAP; ++spin) {
          asm volatile("" ::: "memory");
          const u32x2 p = ld8<SC1>(rw, vraw + h * 1152, 0);
          if (p[1] == tag) { r += __uint_as_float(p[0]); ++h; }
        }
      }
      *(__attribute__((address_space(1))) uint16_t*)po = (uint16_t)pk(r, r);
      if (j == 0 && tid == 0) *reinterpret_cast<u32*>(a.ws + WS_NONCE) = nonce + 1u;
    }
  }
}

}

extern "C" void kernel_launch(void* const* d_in, const int* in_sizes, int n_in, void* d_out, int out_size,
                              void* d_ws, size_t ws_size, hipStream_t stream) {
  Args a;
  a.x = (const uint16_t*)d_in[0]; a.k = (const uint16_t*)d_in[1]; a.v = (const uint16_t*)d_in[2];
  a.wq = (const uint16_t*)d_in[3]; a.wo = (const uint16_t*)d_in[4];
  a.w1 = (const uint16_t*)d_in[6]; a.b1 = (const uint16_t*)d_in[7]; a.w2 = (const uint16_t*)d_in[8];
  a.b2 = (const uint16_t*)d_in[9]; a.gw = (const uint16_t*)d_in[10];
  a.out = (uint16_t*)d_out; a.ws = (unsigned char*)d_ws;
  hipLaunchKernelGGL(fused_decode, dim3(NWG), dim3(NTHR), 0, stream, a);
}
```
